```python
import torch

def _softmax(x, axis=-1):
    return torch.softmax(x, dim=axis)
def _gelu(x):
    return torch.nn.functional.gelu(x, approximate="tanh")
def _silu(x):
    return torch.nn.functional.silu(x)
def _sigmoid(x):
    return torch.sigmoid(x)
def _layernorm(x, axis=-1, eps=1e-5):
    m = x.mean(dim=axis, keepdim=True)
    v = x.var(dim=axis, keepdim=True, unbiased=False)
    return (x - m) / torch.sqrt(v + eps)
def _scatter_add(tgt, idx, src, axis=0):
    return tgt.clone().index_add_(axis, idx, src)
def _segment_sum(src, seg, n):
    out = torch.zeros((n,) + src.shape[1:], dtype=src.dtype)
    return out.index_add_(0, seg, src)
def _pad(x, pw):
    flat = tuple(v for pair in reversed(pw) for v in pair)
    return torch.nn.functional.pad(x, flat)

kernel_name = 'standalone__controlnet__diffusion'

def setup_inputs():

    _w = (lambda a: (lambda t: t.to(torch.bfloat16) if t.is_floating_point() else t)(torch.as_tensor(a)))
    B, HW, C = 2, 32, 512
    sc = C ** -0.5
    return {
        "x_frozen": _w((lambda *s: torch.randn(*s, dtype=torch.bfloat16) * 0.4)(B, HW, HW, C)),
        "x": _w((lambda *s: torch.randn(*s, dtype=torch.bfloat16) * 0.8)(B, HW, HW, C)),
        "c": _w((lambda *s: torch.randn(*s, dtype=torch.bfloat16) * 1.6)(B, HW, HW, C)),
        "w_z1": _w((lambda *s: torch.randn(*s, dtype=torch.bfloat16) * 1.6)(C, C) * sc),
        "w_z2": _w((lambda *s: torch.randn(*s, dtype=torch.bfloat16) * 0.8)(C, C) * sc),
        "w_conv": _w((lambda *s: torch.randn(*s, dtype=torch.bfloat16) * 0.8)(3, 3, C, C) * sc / 3.0),
    }

def reference(x_frozen, x, c, w_z1, w_z2, w_conv):
    B, H, W, C = x.shape
    h_in = x + torch.einsum("bhwc,cd->bhwd", c, w_z1)
    h = _silu(_layernorm(h_in, axis=-1))
    hp = _pad(h, ((0, 0), (1, 1), (1, 1), (0, 0)))
    acc = h * 0.0
    for di in range(3):
        for dj in range(3):
            acc = acc + torch.einsum("bhwc,cd->bhwd", hp[:, di:di + H, dj:dj + W, :], w_conv[di, dj])
    h = h_in + acc
    return x_frozen + torch.einsum("bhwc,cd->bhwd", h, w_z2)
```

```cpp
#include <hip/hip_runtime.h>
#include <hip/hip_bf16.h>
typedef unsigned short u16;
__device__ __forceinline__ float bf2f(u16 b){ return __builtin_bit_cast(float, (unsigned)b<<16); }
__device__ __forceinline__ u16 f2bf(float f){ unsigned u=__builtin_bit_cast(unsigned,f); u += 0x7FFFu + ((u>>16)&1u); return (u16)(u>>16); }
__global__ __launch_bounds__(512) void k1(const u16* x, const u16* c, const u16* w1, u16* hin, u16* h){
  __shared__ float sc[512]; __shared__ float red[16];
  int p=blockIdx.x, d=threadIdx.x;
  sc[d]=bf2f(c[p*512+d]); __syncthreads();
  float a=0; for(int k=0;k<512;k++) a+=sc[k]*bf2f(w1[k*512+d]);
  float v=bf2f(f2bf(bf2f(x[p*512+d])+a));
  hin[p*512+d]=f2bf(v);
  float s=v; for(int o=32;o;o>>=1) s+=__shfl_xor(s,o); if((d&63)==0) red[d>>6]=s; __syncthreads();
  float m=0; for(int i=0;i<8;i++) m+=red[i]; m*=(1.f/512); __syncthreads();
  float q=(v-m)*(v-m); for(int o=32;o;o>>=1) q+=__shfl_xor(q,o); if((d&63)==0) red[8+(d>>6)]=q; __syncthreads();
  float var=0; for(int i=0;i<8;i++) var+=red[8+i]; var*=(1.f/512);
  float y=(v-m)*rsqrtf(var+1e-5f);
  h[p*512+d]=f2bf(y/(1.f+__expf(-y)));
}
__global__ __launch_bounds__(512) void k2(const u16* hin,const u16* h,const u16* wc,u16* h2){
  __shared__ float sc[9*512];
  int p=blockIdx.x, d=threadIdx.x; int b=p>>10, y=(p>>5)&31, xx=p&31;
  for(int t=0;t<9;t++){int yy=y+t/3-1, x2=xx+t%3-1; float v=0; if(yy>=0&&yy<32&&x2>=0&&x2<32) v=bf2f(h[((b*32+yy)*32+x2)*512+d]); sc[t*512+d]=v;}
  __syncthreads();
  float a=0; for(int k=0;k<4608;k++) a+=sc[k]*bf2f(wc[k*512+d]);
  h2[p*512+d]=f2bf(bf2f(hin[p*512+d])+a);
}
__global__ __launch_bounds__(512) void k3(const u16* xf,const u16* h2,const u16* w2,u16* out){
  __shared__ float sc[512];
  int p=blockIdx.x, d=threadIdx.x;
  sc[d]=bf2f(h2[p*512+d]); __syncthreads();
  float a=0; for(int k=0;k<512;k++) a+=sc[k]*bf2f(w2[k*512+d]);
  out[p*512+d]=f2bf(bf2f(xf[p*512+d])+a);
}
extern "C" void kernel_launch(void* const* d_in, const int* in_sizes, int n_in, void* d_out, int out_size, void* d_ws, size_t ws_size, hipStream_t stream){
  const u16* xf=(const u16*)d_in[0]; const u16* x=(const u16*)d_in[1]; const u16* c=(const u16*)d_in[2];
  const u16* w1=(const u16*)d_in[3]; const u16* w2=(const u16*)d_in[4]; const u16* wc=(const u16*)d_in[5];
  u16* hin=(u16*)d_ws; u16* h=hin+2048*512; u16* h2=h+2048*512;
  k1<<<2048,512,0,stream>>>(x,c,w1,hin,h);
  k2<<<2048,512,0,stream>>>(hin,h,wc,h2);
  k3<<<2048,512,0,stream>>>(xf,h2,w2,(u16*)d_out);
}
```

```cpp
#include <hip/hip_runtime.h>
#include <hip/hip_bf16.h>
typedef unsigned short u16;
typedef unsigned v4u __attribute__((ext_vector_type(4)));
typedef unsigned v2u __attribute__((ext_vector_type(2)));
typedef short v4s __attribute__((ext_vector_type(4)));
typedef short v8s __attribute__((ext_vector_type(8)));
typedef __bf16 v8bf __attribute__((ext_vector_type(8)));
typedef __bf16 v2bf __attribute__((ext_vector_type(2)));
typedef float f32x2 __attribute__((ext_vector_type(2)));
typedef float f32x4 __attribute__((ext_vector_type(4)));
typedef float f32x16 __attribute__((ext_vector_type(16)));
#define GAS __attribute__((address_space(1)))
#define LAS __attribute__((address_space(3)))
#define DI __device__ __forceinline__
#define RLX_AGENT __ATOMIC_RELAXED, __HIP_MEMORY_SCOPE_AGENT

constexpr int WS_EPOCH = 0;
constexpr int WS_STATS = 16384;
constexpr int WS_HN = WS_STATS + 262144;
constexpr int WS_HIN = WS_HN + 2097152;
constexpr int WS_H2 = WS_HIN + 2097152;
constexpr int WS_WCT = WS_H2 + 2097152;
constexpr int WS_W2T = WS_WCT + 4718592;
constexpr int WS_END = WS_W2T + 524288;
constexpr int RSRC_W3 = 0x00020000;
constexpr int AUX_SC1 = 16;

#define STAMP(slot)

DI unsigned pk_bf16(float a, float b) { f32x2 v = {a, b}; return __builtin_bit_cast(unsigned, __builtin_convertvector(v, v2bf)); }
DI float bf_lo(unsigned u) { return __builtin_bit_cast(float, u << 16); }
DI float bf_hi(unsigned u) { return __builtin_bit_cast(float, u & 0xffff0000u); }
DI f32x4 mfma16(v4u a, v4u b, f32x4 c) { return __builtin_amdgcn_mfma_f32_16x16x32_bf16(__builtin_bit_cast(v8bf, a), __builtin_bit_cast(v8bf, b), c, 0, 0, 0); }
DI f32x16 mfma32(v4u a, v4u b, f32x16 c) { return __builtin_amdgcn_mfma_f32_32x32x16_bf16(__builtin_bit_cast(v8bf, a), __builtin_bit_cast(v8bf, b), c, 0, 0, 0); }
DI float asf(unsigned u) { return __builtin_bit_cast(float, u); }
DI unsigned asu(float f) { return __builtin_bit_cast(unsigned, f); }
DI float xsum16(float v) { auto r = __builtin_amdgcn_permlane16_swap(asu(v), asu(v), false, false); return asf(r[0]) + asf(r[1]); }
DI float xsum32(float v) { auto r = __builtin_amdgcn_permlane32_swap(asu(v), asu(v), false, false); return asf(r[0]) + asf(r[1]); }
#define BARRIER() do { asm volatile("" ::: "memory"); __builtin_amdgcn_s_barrier(); asm volatile("" ::: "memory"); } while (0)
template <int N> DI void wait_vm() { asm volatile("s_waitcnt vmcnt(%0)" ::"i"(N) : "memory"); }
DI void glds16(const void* g, LAS void* l) { __builtin_amdgcn_global_load_lds((const GAS unsigned*)g, (LAS unsigned*)l, 16, 0, 0); }

constexpr int K1_C = 0;
constexpr int K1_W = 65536;
constexpr int K1_X = 131072;
constexpr int K1_LDS = 139264;

__global__ __launch_bounds__(512, 2) void k1(const u16* __restrict__ x, const u16* __restrict__ c, const u16* __restrict__ w1,
                                             const u16* __restrict__ wc, const u16* __restrict__ w2, char* ws) {
  extern __shared__ __attribute__((aligned(16))) char smem_[];
  LAS char* smem = (LAS char*)smem_;
  const int bid = blockIdx.x, lane = threadIdx.x & 63, w = __builtin_amdgcn_readfirstlane(threadIdx.x >> 6);
  const int pt = (bid & 7) * 4 + (bid >> 6), ct = (bid >> 3) & 7, p0 = pt * 64;
  STAMP(0);
  if (w >= 4) {
    const int hw = w - 4;
    const int r8 = lane >> 3, s8 = lane & 7;
    const char* cs0 = (const char*)c + (size_t)(p0 + 16 * hw + r8) * 1024 + ((s8 ^ (r8 >> 1)) << 4);
    const char* cs1 = (const char*)c + (size_t)(p0 + 16 * hw + 8 + r8) * 1024 + ((s8 ^ (4 + (r8 >> 1))) << 4);
    const char* ws0 = (const char*)w1 + (size_t)(16 * hw + r8) * 1024 + ct * 128 + ((s8 ^ (r8 & 2)) << 4);
    const char* ws1 = (const char*)w1 + (size_t)(16 * hw + 8 + r8) * 1024 + ct * 128 + ((s8 ^ (r8 & 2) ^ 4) << 4);
#pragma unroll
    for (int kc = 0; kc < 8; ++kc) {
      glds16(cs0 + kc * 128, smem + K1_C + kc * 8192 + hw * 2048);
      glds16(cs1 + kc * 128, smem + K1_C + kc * 8192 + hw * 2048 + 1024);
      glds16(ws0 + kc * 65536, smem + K1_W + kc * 8192 + hw * 2048);
      glds16(ws1 + kc * 65536, smem + K1_W + kc * 8192 + hw * 2048 + 1024);
    }
    glds16((const char*)x + (size_t)(p0 + 16 * hw + r8) * 1024 + ct * 128 + ((s8 ^ (r8 >> 1)) << 4), smem + K1_X + hw * 2048);
    glds16((const char*)x + (size_t)(p0 + 16 * hw + 8 + r8) * 1024 + ct * 128 + ((s8 ^ (4 + (r8 >> 1))) << 4), smem + K1_X + hw * 2048 + 1024);
    unsigned rt[3][8];
#pragma unroll
    for (int j = 0; j < 3; ++j) {
      int uj = hw * 3 + j; uj = uj > 9 ? 9 : uj;
      const int u = bid * 10 + uj, rg = u >> 2, cq = u & 3;
      const char* src = (rg < 576 ? (const char*)wc + (size_t)rg * 8192 : (const char*)w2 + (size_t)(rg - 576) * 8192) + cq * 256 + lane * 4;
#pragma unroll
      for (int i = 0; i < 8; ++i) rt[j][i] = *(const GAS unsigned*)(src + i * 1024);
    }
    __builtin_amdgcn_sched_barrier(0);
    wait_vm<54>(); BARRIER();
    wait_vm<50>(); BARRIER();
    wait_vm<46>(); BARRIER();
    wait_vm<42>(); BARRIER();
    wait_vm<38>(); BARRIER();
    wait_vm<34>(); BARRIER();
    wait_vm<30>(); BARRIER();
    wait_vm<24>(); BARRIER();
    __builtin_amdgcn_sched_barrier(0);
#pragma unroll
    for (int j = 0; j < 3; ++j) {
      int uj = hw * 3 + j; uj = uj > 9 ? 9 : uj;
      const int u = bid * 10 + uj, rg = u >> 2, cq = u & 3;
      const int n = 4 * cq + (lane >> 4);
      const int frag = rg < 576 ? (WS_WCT >> 10) + (n * 32 + ((rg & 63) >> 1)) * 9 + (rg >> 6) : (WS_W2T >> 10) + n * 32 + ((rg - 576) >> 1);
      char* dst = ws + (size_t)frag * 1024 + (rg & 1) * 512 + (lane & 15) * 32;
      v4u lo, hi;
#pragma unroll
      for (int i = 0; i < 4; ++i) { lo[i] = __builtin_amdgcn_perm(rt[j][2 * i + 1], rt[j][2 * i], 0x05040100u); hi[i] = __builtin_amdgcn_perm(rt[j][2 * i + 1], rt[j][2 * i], 0x07060302u); }
      *(GAS v4u*)dst = lo; *(GAS v4u*)(dst + 16) = hi;
    }
    STAMP(1);
    return;
  }
  const unsigned tag = __hip_atomic_load((GAS unsigned*)(ws + WS_EPOCH + pt * 256), RLX_AGENT) + 1u;
  const int px = lane & 15, g = lane >> 4, q = (lane & 15) >> 2, p = lane & 3;
  const int row = 16 * w + px;
  LAS char* cb0 = smem + K1_C + row * 128 + ((g ^ ((px >> 1) & 7)) << 4);
  LAS char* cb1 = smem + K1_C + row * 128 + (((4 + g) ^ ((px >> 1) & 7)) << 4);
  LAS char* wb[4];
#pragma unroll
  for (int cc = 0; cc < 4; ++cc) wb[cc] = smem + K1_W + (8 * g + q) * 128 + (((2 * cc + (p >> 1)) ^ ((q & 2) | ((g & 1) << 2))) << 4) + 8 * (p & 1);
  f32x4 acc[4] = {};
  v4u bq[2][2], aq[2][2][4];
#pragma unroll
  for (int kc = 0; kc <= 8; ++kc) {
    if (kc < 8) {
      BARRIER();
#pragma unroll
      for (int s = 0; s < 2; ++s) {
        bq[kc & 1][s] = *(LAS v4u*)((s ? cb1 : cb0) + kc * 8192);
#pragma unroll
        for (int cc = 0; cc < 4; ++cc) {
          v4s a0 = __builtin_amdgcn_ds_read_tr16_b64_v4i16((LAS v4s*)(wb[cc] + kc * 8192 + s * 4096));
          v4s a1 = __builtin_amdgcn_ds_read_tr16_b64_v4i16((LAS v4s*)(wb[cc] + kc * 8192 + s * 4096 + 512));
          aq[kc & 1][s][cc] = __builtin_bit_cast(v4u, __builtin_shufflevector(a0, a1, 0, 1, 2, 3, 4, 5, 6, 7));
        }
      }
      __builtin_amdgcn_sched_barrier(0);
    }
    if (kc > 0) {
#pragma unroll
      for (int s = 0; s < 2; ++s)
#pragma unroll
        for (int cc = 0; cc < 4; ++cc) acc[cc] = mfma16(aq[(kc - 1) & 1][s][cc], bq[(kc - 1) & 1][s], acc[cc]);
      __builtin_amdgcn_sched_barrier(0);
    }
  }
  STAMP(2);
  float v[16]; v2u hin[4];
#pragma unroll
  for (int cc = 0; cc < 4; ++cc) {
    v2u xv = *(LAS v2u*)(smem + K1_X + row * 128 + (((2 * cc + (g >> 1)) ^ ((px >> 1) & 7)) << 4) + 8 * (g & 1));
    hin[cc].x = pk_bf16(acc[cc][0] + bf_lo(xv.x), acc[cc][1] + bf_hi(xv.x));
    hin[cc].y = pk_bf16(acc[cc][2] + bf_lo(xv.y), acc[cc][3] + bf_hi(xv.y));
    v[4 * cc] = bf_lo(hin[cc].x); v[4 * cc + 1] = bf_hi(hin[cc].x); v[4 * cc + 2] = bf_lo(hin[cc].y); v[4 * cc + 3] = bf_hi(hin[cc].y);
  }
  float s = 0.f;
#pragma unroll
  for (int i = 0; i < 16; ++i) s += v[i];
  const float m64 = xsum32(xsum16(s)) * (1.f / 64.f);
  float q2 = 0.f;
#pragma unroll
  for (int i = 0; i < 16; ++i) { float d = v[i] - m64; q2 += d * d; }
  q2 = xsum32(xsum16(q2));
  const __amdgpu_buffer_rsrc_t rs = __builtin_amdgcn_make_buffer_rsrc((void*)ws, 0, WS_END, RSRC_W3);
  const int soff = WS_STATS + pt * 8192 + row * 16;
  if (g == 0) { v4u gr = {__builtin_bit_cast(unsigned, m64), tag, __builtin_bit_cast(unsigned, q2), tag}; __builtin_amdgcn_raw_buffer_store_b128(gr, rs, soff + ct * 1024, 0, AUX_SC1); }
  const int poff = ((8 * ct + (g >> 1)) * 2048 + p0 + row) * 16 + 8 * (g & 1);
#pragma unroll
  for (int cc = 0; cc < 4; ++cc) __builtin_amdgcn_raw_buffer_store_b64(hin[cc], rs, poff + cc * 65536, WS_HIN, 0);
  STAMP(3);
  v4u g0, g1;
  for (unsigned spin = 0;; ++spin) {
    asm volatile("" ::: "memory");
    g0 = __builtin_amdgcn_raw_buffer_load_b128(rs, soff + g * 2048, 0, AUX_SC1);
    g1 = __builtin_amdgcn_raw_buffer_load_b128(rs, soff + g * 2048 + 1024, 0, AUX_SC1);
    const bool ok = (g0.y == tag) & (g0.w == tag) & (g1.y == tag) & (g1.w == tag);
    if (__all(ok) || spin > (1u << 20)) break;
  }
  STAMP(4);
  if (ct == 0 && w == 0 && lane == 0) __hip_atomic_store((GAS unsigned*)(ws + WS_EPOCH + pt * 256), tag, RLX_AGENT);
  const float ma = asf(g0.x), mb = asf(g1.x);
  const float mean = xsum32(xsum16(ma + mb)) * 0.125f;
  const float da = ma - mean, db = mb - mean;
  const float M2 = xsum32(xsum16(asf(g0.z) + asf(g1.z) + 64.f * (da * da + db * db)));
  const float rstd = __builtin_amdgcn_rsqf(M2 * (1.f / 512.f) + 1e-5f);
  const float nb = -mean * rstd;
#pragma unroll
  for (int cc = 0; cc < 4; ++cc) {
    float o[4];
#pragma unroll
    for (int i = 0; i < 4; ++i) { float y = v[4 * cc + i] * rstd + nb; o[i] = y * __builtin_amdgcn_rcpf(1.f + __builtin_amdgcn_exp2f(y * -1.44269504f)); }
    v2u hv = {pk_bf16(o[0], o[1]), pk_bf16(o[2], o[3])};
    __builtin_amdgcn_raw_buffer_store_b64(hv, rs, poff + cc * 65536, WS_HN, 0);
  }
  STAMP(5);
}

constexpr int K2_ST = 4;
__global__ __launch_bounds__(256, 1) void k2(char* ws) {
  extern __shared__ __attribute__((aligned(16))) char smem_[];
  LAS char* smem = (LAS char*)smem_;
  const int bid = blockIdx.x, lane = threadIdx.x & 63, w = __builtin_amdgcn_readfirstlane(threadIdx.x >> 6);
  const int xcd = bid & 7, idx = bid >> 3, b = xcd >> 2, rt = idx >> 2, n = (xcd & 3) * 4 + (idx & 3);
  STAMP(8);
  const __amdgpu_buffer_rsrc_t rs = __builtin_amdgcn_make_buffer_rsrc((void*)ws, 0, WS_END, RSRC_W3);
  const int r = lane & 31, h = lane >> 5;
  const int vw = lane * 16;
  const int va = h * 32768 + r * 16;
  const int va_top = rt == 0 ? 0x40000000 : va, va_bot = rt == 7 ? 0x40000000 : va;
  const int wbase = WS_WCT + (n * 32 + 8 * w) * 9216;
  const int abase = WS_HN + 8 * w * 65536 + (b * 1024 + (4 * rt - 1) * 32) * 16;
  unsigned mlo = r == 0 ? 0u : ~0u, mhi = r == 31 ? 0u : ~0u;
  asm volatile("" : "+v"(mlo), "+v"(mhi));
  v4u wf[K2_ST][9], af[K2_ST][6];
  auto issue = [&](int j, int st) {
    const int cj = (j + rt) & 7;
    const int so_a = abase + cj * 65536, so_w = wbase + cj * 9216;
    af[st][0] = __builtin_amdgcn_raw_buffer_load_b128(rs, va_top, so_a, 0);
    wf[st][0] = __builtin_amdgcn_raw_buffer_load_b128(rs, vw, so_w, 0);
    wf[st][1] = __builtin_amdgcn_raw_buffer_load_b128(rs, vw + 1024, so_w, 0);
    wf[st][2] = __builtin_amdgcn_raw_buffer_load_b128(rs, vw + 2048, so_w, 0);
    af[st][1] = __builtin_amdgcn_raw_buffer_load_b128(rs, va + 512, so_a, 0);
    wf[st][3] = __builtin_amdgcn_raw_buffer_load_b128(rs, vw + 3072, so_w, 0);
    wf[st][4] = __builtin_amdgcn_raw_buffer_load_b128(rs, vw, so_w + 4096, 0);
    wf[st][5] = __builtin_amdgcn_raw_buffer_load_b128(rs, vw + 1024, so_w + 4096, 0);
    af[st][2] = __builtin_amdgcn_raw_buffer_load_b128(rs, va + 1024, so_a, 0);
    wf[st][6] = __builtin_amdgcn_raw_buffer_load_b128(rs, vw + 2048, so_w + 4096, 0);
    wf[st][7] = __builtin_amdgcn_raw_buffer_load_b128(rs, vw + 3072, so_w + 4096, 0);
    wf[st][8] = __builtin_amdgcn_raw_buffer_load_b128(rs, vw, so_w + 8192, 0);
    af[st][3] = __builtin_amdgcn_raw_buffer_load_b128(rs, va + 1536, so_a, 0);
    af[st][4] = __builtin_amdgcn_raw_buffer_load_b128(rs, va + 2048, so_a, 0);
    af[st][5] = __builtin_amdgcn_raw_buffer_load_b128(rs, va_bot + 2560, so_a, 0);
  };
#pragma unroll
  for (int j = 0; j < K2_ST; ++j) issue(j, j);
  __builtin_amdgcn_sched_barrier(0);
  f32x16 acc[4] = {};
#pragma unroll
  for (int j = 0; j < 8; ++j) {
    const int st = j % K2_ST;
#pragma unroll
    for (int R = 0; R < 6; ++R) {
      v4u a1 = af[st][R], a0, a2;
#pragma unroll
      for (int i = 0; i < 4; ++i) {
        a0[i] = __builtin_amdgcn_update_dpp(0u, a1[i], 0x138, 0xf, 0xf, true) & mlo;
        a2[i] = __builtin_amdgcn_update_dpp(0u, a1[i], 0x130, 0xf, 0xf, true) & mhi;
      }
#pragma unroll
      for (int dy = 0; dy < 3; ++dy) {
        const int o = R - dy;
        if (o >= 0 && o < 4) {
          acc[o] = mfma32(wf[st][3 * dy + 1], a1, acc[o]);
          acc[o] = mfma32(wf[st][3 * dy + 0], a0, acc[o]);
          acc[o] = mfma32(wf[st][3 * dy + 2], a2, acc[o]);
        }
      }
    }
    __builtin_amdgcn_sched_barrier(0);
    if (j + K2_ST < 8) issue(j + K2_ST, st);
    __builtin_amdgcn_sched_barrier(0);
  }
  STAMP(9);
  const int eoff = ((4 * n) * 2048 + b * 1024 + (4 * rt + w) * 32 + r) * 16 + 8 * h;
  v2u hin[4];
#pragma unroll
  for (int g = 0; g < 4; ++g) hin[g] = __builtin_amdgcn_raw_buffer_load_b64(rs, eoff + g * 32768, WS_HIN, 0);
#pragma unroll
  for (int o = 0; o < 4; ++o)
#pragma unroll
    for (int g = 0; g < 4; ++g) { f32x4 t = {acc[o][4 * g], acc[o][4 * g + 1], acc[o][4 * g + 2], acc[o][4 * g + 3]}; *(LAS f32x4*)(smem + ((w * 4 + o) * 4 + g) * 1024 + lane * 16) = t; }
  __syncthreads();
#pragma unroll
  for (int g = 0; g < 4; ++g) {
    f32x4 t = *(LAS f32x4*)(smem + ((0 * 4 + w) * 4 + g) * 1024 + lane * 16);
#pragma unroll
    for (int s = 1; s < 4; ++s) t += *(LAS f32x4*)(smem + ((s * 4 + w) * 4 + g) * 1024 + lane * 16);
    v2u o = {pk_bf16(t[0] + bf_lo(hin[g].x), t[1] + bf_hi(hin[g].x)), pk_bf16(t[2] + bf_lo(hin[g].y), t[3] + bf_hi(hin[g].y))};
    __builtin_amdgcn_raw_buffer_store_b64(o, rs, eoff + g * 32768, WS_H2, 0);
  }
  STAMP(10);
}

__global__ __launch_bounds__(256, 1) void k3(const u16* __restrict__ xf, char* ws, u16* __restrict__ out) {
  extern __shared__ __attribute__((aligned(16))) char smem_[];
  LAS char* smem = (LAS char*)smem_;
  const int bid = blockIdx.x, lane = threadIdx.x & 63, w = __builtin_amdgcn_readfirstlane(threadIdx.x >> 6);
  const int pt = (bid & 7) * 4 + (bid >> 6), ct = (bid >> 3) & 7, p0 = pt * 64;
  STAMP(16);
  const __amdgpu_buffer_rsrc_t rs = __builtin_amdgcn_make_buffer_rsrc((void*)ws, 0, WS_END, RSRC_W3);
  const int r = lane & 31, h = lane >> 5;
  const int vw = lane * 16, va = h * 32768 + r * 16;
  const int wbase = WS_W2T + (2 * ct * 32 + 8 * w) * 1024;
  const int abase = WS_H2 + 8 * w * 65536 + p0 * 16;
  v4u wf[2][8], af[2][8];
#pragma unroll
  for (int s = 0; s < 8; ++s) {
    af[0][s] = __builtin_amdgcn_raw_buffer_load_b128(rs, va, abase + s * 65536, 0);
    wf[0][s] = __builtin_amdgcn_raw_buffer_load_b128(rs, vw, wbase + s * 1024, 0);
    af[1][s] = __builtin_amdgcn_raw_buffer_load_b128(rs, va + 512, abase + s * 65536, 0);
    wf[1][s] = __builtin_amdgcn_raw_buffer_load_b128(rs, vw, wbase + 32768 + s * 1024, 0);
  }
  const size_t ooff = (size_t)(p0 + 32 * (w >> 1) + r) * 1024 + ct * 128 + (w & 1) * 64 + 8 * h;
  v2u xv[4];
#pragma unroll
  for (int g = 0; g < 4; ++g) xv[g] = *(const GAS v2u*)((const char*)xf + ooff + g * 16);
  __builtin_amdgcn_sched_barrier(0);
  f32x16 acc[4] = {};
#pragma unroll
  for (int s = 0; s < 8; ++s) {
    acc[0] = mfma32(wf[0][s], af[0][s], acc[0]);
    acc[2] = mfma32(wf[0][s], af[1][s], acc[2]);
    acc[1] = mfma32(wf[1][s], af[0][s], acc[1]);
    acc[3] = mfma32(wf[1][s], af[1][s], acc[3]);
  }
  STAMP(17);
#pragma unroll
  for (int o = 0; o < 4; ++o)
#pragma unroll
    for (int g = 0; g < 4; ++g) { f32x4 t = {acc[o][4 * g], acc[o][4 * g + 1], acc[o][4 * g + 2], acc[o][4 * g + 3]}; *(LAS f32x4*)(smem + ((w * 4 + o) * 4 + g) * 1024 + lane * 16) = t; }
  __syncthreads();
#pragma unroll
  for (int g = 0; g < 4; ++g) {
    f32x4 t = *(LAS f32x4*)(smem + ((0 * 4 + w) * 4 + g) * 1024 + lane * 16);
#pragma unroll
    for (int s = 1; s < 4; ++s) t += *(LAS f32x4*)(smem + ((s * 4 + w) * 4 + g) * 1024 + lane * 16);
    v2u o = {pk_bf16(t[0] + bf_lo(xv[g].x), t[1] + bf_hi(xv[g].x)), pk_bf16(t[2] + bf_lo(xv[g].y), t[3] + bf_hi(xv[g].y))};
    *(GAS v2u*)((char*)out + ooff + g * 16) = o;
  }
  STAMP(18);
}

extern "C" void kernel_launch(void* const* d_in, const int* in_sizes, int n_in, void* d_out, int out_size, void* d_ws, size_t ws_size, hipStream_t stream) {
  const u16* xf = (const u16*)d_in[0]; const u16* x = (const u16*)d_in[1]; const u16* c = (const u16*)d_in[2];
  const u16* w1 = (const u16*)d_in[3]; const u16* w2 = (const u16*)d_in[4]; const u16* wc = (const u16*)d_in[5];
  char* ws = (char*)d_ws;
  (void)hipFuncSetAttribute((const void*)k1, hipFuncAttributeMaxDynamicSharedMemorySize, K1_LDS);
  k1<<<256, 512, K1_LDS, stream>>>(x, c, w1, wc, w2, ws);
  k2<<<256, 256, 65536, stream>>>(ws);
  k3<<<256, 256, 65536, stream>>>(xf, ws, (u16*)d_out);
}
```

```cpp
#define XPRE 22
#include <hip/hip_runtime.h>
#include <hip/hip_bf16.h>
typedef unsigned short u16;
typedef unsigned v4u __attribute__((ext_vector_type(4)));
typedef unsigned v2u __attribute__((ext_vector_type(2)));
typedef short v4s __attribute__((ext_vector_type(4)));
typedef __bf16 v8bf __attribute__((ext_vector_type(8)));
typedef __bf16 v2bf __attribute__((ext_vector_type(2)));
typedef float f32x2 __attribute__((ext_vector_type(2)));
typedef float f32x4 __attribute__((ext_vector_type(4)));
#define GAS __attribute__((address_space(1)))
#define LAS __attribute__((address_space(3)))
#define DI __device__ __forceinline__
#define AI __attribute__((always_inline))
#define SB() __builtin_amdgcn_sched_barrier(0)
#define CB() asm volatile("" ::: "memory")

constexpr int WS_EPOCH = 0;
constexpr int WS_STATS = 16384;
constexpr int WS_HN = WS_STATS + 262144;
constexpr int WS_HIN = WS_HN + 2097152;
constexpr int WS_H2 = WS_HIN + 2097152;
constexpr int WS_WCT = WS_H2 + 2097152;
constexpr int WS_W2T = WS_WCT + 4718592;
constexpr int WS_END = WS_W2T + 524288;
constexpr int RSRC_W3 = 0x00020000;
constexpr int SC1 = 16;
constexpr unsigned SPIN_CAP = 1u << 17;
constexpr unsigned POISON = 0xFFFFFFFFu;

DI float asf(unsigned u) { return __builtin_bit_cast(float, u); }
DI unsigned asu(float f) { return __builtin_bit_cast(unsigned, f); }
DI unsigned pk_bf16(float a, float b) { f32x2 v = {a, b}; return __builtin_bit_cast(unsigned, __builtin_convertvector(v, v2bf)); }
DI float bf_lo(unsigned u) { return asf(u << 16); }
DI float bf_hi(unsigned u) { return asf(u & 0xffff0000u); }
DI unsigned umin(unsigned a, unsigned b) { return a < b ? a : b; }
DI unsigned umax(unsigned a, unsigned b) { return a > b ? a : b; }
DI f32x4 mfma16(v4u a, v4u b, f32x4 c) { return __builtin_amdgcn_mfma_f32_16x16x32_bf16(__builtin_bit_cast(v8bf, a), __builtin_bit_cast(v8bf, b), c, 0, 0, 0); }
DI float xsum16(float v) { auto r = __builtin_amdgcn_permlane16_swap(asu(v), asu(v), false, false); return asf(r[0]) + asf(r[1]); }
DI float xsum32(float v) { auto r = __builtin_amdgcn_permlane32_swap(asu(v), asu(v), false, false); return asf(r[0]) + asf(r[1]); }
DI v4u widen16(v2u a, v2u b) {
  auto rx = __builtin_amdgcn_permlane16_swap(a.x, b.x, false, false);
  auto ry = __builtin_amdgcn_permlane16_swap(a.y, b.y, false, false);
  v4u o = {rx[0], ry[0], rx[1], ry[1]};
  return o;
}
DI v4u transpose4(unsigned a0, unsigned a1, unsigned a2, unsigned a3) {
  auto p = __builtin_amdgcn_permlane16_swap(a0, a1, false, false);
  auto q = __builtin_amdgcn_permlane16_swap(a2, a3, false, false);
  auto r = __builtin_amdgcn_permlane32_swap(p[0], q[0], false, false);
  auto s = __builtin_amdgcn_permlane32_swap(p[1], q[1], false, false);
  v4u o = {r[0], s[0], r[1], s[1]};
  return o;
}
DI v4u trfrag(LAS char* p0, LAS char* p1) {
  v4s a0 = __builtin_amdgcn_ds_read_tr16_b64_v4i16((LAS v4s*)p0);
  v4s a1 = __builtin_amdgcn_ds_read_tr16_b64_v4i16((LAS v4s*)p1);
  return __builtin_bit_cast(v4u, __builtin_shufflevector(a0, a1, 0, 1, 2, 3, 4, 5, 6, 7));
}
template <int V> struct IC { static constexpr int v = V; };
template <int B, int E, class F> DI void static_for(F&& f) { if constexpr (B < E) { f(IC<B>{}); static_for<B + 1, E>(f); } }
#define BARRIER() do { CB(); __builtin_amdgcn_s_barrier(); CB(); } while (0)
template <int N> DI void wait_vm() { asm volatile("s_waitcnt vmcnt(%0)" ::"i"(N) : "memory"); }
DI void wait_lgkm0() { asm volatile("s_waitcnt lgkmcnt(0)" ::: "memory"); }
DI v4u mk_rsrc(const void* p, unsigned bytes) { unsigned long long a = (unsigned long long)p; v4u r = {(unsigned)a, (unsigned)(a >> 32) & 0xffffu, bytes, (unsigned)RSRC_W3}; return r; }
DI void dma16(v4u rsrc, int voff, int soff, int lds) {
  asm volatile("s_mov_b32 m0, %0\n\ts_nop 0\n\tbuffer_load_dwordx4 %1, %2, %3 offen lds" ::"s"(lds), "v"(voff), "s"(rsrc), "s"(soff) : "memory");
}
#define ST128(val, rs, v, aux) __builtin_amdgcn_raw_buffer_store_b128(val, rs, v, 0, aux)
#define LD128(rs, v, s, aux) __builtin_amdgcn_raw_buffer_load_b128(rs, v, s, aux)
#define ST32(val, rs, v, aux) __builtin_amdgcn_raw_buffer_store_b32(val, rs, v, 0, aux)

constexpr int L_C = 0;
constexpr int L_W = 65536;
constexpr int L_X = 131072;
constexpr int K1_LDS = 139264;
#define R_AT 4
#define R_PO 4
extern "C" __global__ __launch_bounds__(832, 4) void cn_k1(const u16* __restrict__ x, const u16* __restrict__ c, const u16* __restrict__ w1,
                                                           const u16* __restrict__ wc, const u16* __restrict__ w2, char* ws) {
  extern __shared__ __attribute__((aligned(16))) char smem_[];
  LAS char* smem = (LAS char*)smem_;
  const int bid = blockIdx.x, lane = threadIdx.x & 63, wv = __builtin_amdgcn_readfirstlane(threadIdx.x >> 6);
  const int idx = bid >> 3, pt = (bid & 7) * 4 + (idx >> 3), ct = idx & 7;
  const __amdgpu_buffer_rsrc_t rs = __builtin_amdgcn_make_buffer_rsrc((void*)ws, 0, WS_END, RSRC_W3);
  if (__builtin_expect(wv < 8, 1)) {
    const int w = wv & 3;
    const int r8 = lane >> 3, s8 = lane & 7;
    const int voc = (pt * 64 + (w >> 1) + 2 * (8 * (w & 1) + r8)) * 1024 + ((s8 ^ (4 * (w & 1) + (r8 >> 1))) << 4);
    const int vow = (8 * w + r8) * 1024 + ct * 128 + ((s8 ^ (r8 & 2) ^ ((w & 1) << 2)) << 4);
    const int rot = 4 * ((ct ^ (idx >> 3)) & 1);
    const v4u rc = mk_rsrc(c, 2097152), rw1 = mk_rsrc(w1, 524288);
    {
      const int hi = (wv >> 2) * 32768;
#pragma unroll
      for (int i = 0; i < 4; ++i) {
        const int kc = i ^ rot;
        dma16(rc, voc, hi + kc * 128, L_C + i * 8192 + wv * 1024);
        dma16(rw1, vow, hi + kc * 65536, L_W + i * 8192 + wv * 1024);
      }
    }
    if (__builtin_expect(wv >= 4, 1)) {
      auto issue = [&](int i) {
        const int kc = i ^ rot;
        dma16(rc, voc, kc * 128, L_C + i * 8192 + w * 1024);
        dma16(rw1, vow, kc * 65536, L_W + i * 8192 + w * 1024);
        dma16(rc, voc, 32768 + kc * 128, L_C + i * 8192 + 4096 + w * 1024);
        dma16(rw1, vow, 32768 + kc * 65536, L_W + i * 8192 + 4096 + w * 1024);
      };
      const v4u rx = mk_rsrc(x, 2097152);
      wait_vm<6>(); BARRIER(); issue(4);
      wait_vm<8>(); BARRIER(); issue(5);
      wait_vm<10>(); BARRIER(); issue(6);
      wait_vm<12>(); BARRIER(); issue(7);
      dma16(rx, voc, ct * 128, L_X + w * 1024);
      dma16(rx, voc, 32768 + ct * 128, L_X + 4096 + w * 1024);
      wait_vm<14>(); BARRIER();
      wait_vm<10>(); BARRIER();
      wait_vm<6>(); BARRIER();
      wait_vm<0>(); BARRIER();
      return;
    }
    const int px = lane & 15, g = lane >> 4, q = (lane & 15) >> 2, p = lane & 3;
    const int row = 16 * w + px;
    const int soff = WS_STATS + pt * 8192 + row * 16;
    unsigned epoch_;
    asm volatile("s_load_dword %0, %1, %2" : "=&s"(epoch_) : "s"(ws), "s"(WS_EPOCH + (pt * 4 + w) * 128) : "memory");
    LAS char* cb0 = smem + L_C + row * 128 + ((g ^ ((px >> 1) & 7)) << 4);
    LAS char* cb1 = smem + L_C + row * 128 + (((4 + g) ^ ((px >> 1) & 7)) << 4);
    LAS char* wb[4];
#pragma unroll
    for (int cc = 0; cc < 4; ++cc) wb[cc] = smem + L_W + (8 * g + q) * 128 + (((2 * cc + (p >> 1)) ^ ((q & 2) | ((g & 1) << 2))) << 4) + 8 * (p & 1);
    f32x4 acc[4] = {};
    v4u bq[2][2], aq[2][2][4];
    v2u xv[4];
#pragma unroll
    for (int kc = 0; kc <= 8; ++kc) {
      if (kc == 0) { asm volatile("s_waitcnt vmcnt(6) lgkmcnt(0)" : "+s"(epoch_) :: "memory"); }
      if (kc == 1) wait_vm<4>();
      if (kc == 2) wait_vm<2>();
      if (kc == 3) wait_vm<0>();
      if (kc < 8) BARRIER();
#pragma unroll
      for (int s = 0; s < 2; ++s) {
        if (kc < 8) bq[kc & 1][s] = *(LAS v4u*)((s ? cb1 : cb0) + kc * 8192);
#pragma unroll
        for (int cc = 0; cc < 4; ++cc) {
          if (kc < 8) aq[kc & 1][s][cc] = trfrag(wb[cc] + kc * 8192 + s * 4096, wb[cc] + kc * 8192 + s * 4096 + 512);
          if (kc == 7 && s == 1) xv[cc] = *(LAS v2u*)(smem + L_X + row * 128 + (((2 * cc + (g >> 1)) ^ ((px >> 1) & 7)) << 4) + 8 * (g & 1));
          if (kc > 0) acc[cc] = mfma16(aq[(kc - 1) & 1][s][cc], bq[(kc - 1) & 1][s], acc[cc]);
          SB();
        }
      }
    }
    const unsigned tag = epoch_ + 1u;
    float v[16]; v2u hin[4];
    float sa = 0.f, sb = 0.f, qa = 0.f, qb = 0.f;
#pragma unroll
    for (int cc = 0; cc < 4; ++cc) {
      hin[cc].x = pk_bf16(acc[cc][0] + bf_lo(xv[cc].x), acc[cc][1] + bf_hi(xv[cc].x));
      hin[cc].y = pk_bf16(acc[cc][2] + bf_lo(xv[cc].y), acc[cc][3] + bf_hi(xv[cc].y));
      v[4 * cc] = bf_lo(hin[cc].x); v[4 * cc + 1] = bf_hi(hin[cc].x); v[4 * cc + 2] = bf_lo(hin[cc].y); v[4 * cc + 3] = bf_hi(hin[cc].y);
      sa += v[4 * cc] + v[4 * cc + 2]; sb += v[4 * cc + 1] + v[4 * cc + 3];
      qa += v[4 * cc] * v[4 * cc]; qb += v[4 * cc + 1] * v[4 * cc + 1]; qa += v[4 * cc + 2] * v[4 * cc + 2]; qb += v[4 * cc + 3] * v[4 * cc + 3];
    }
    const float s64 = xsum32(xsum16(sa + sb)), q64 = xsum32(xsum16(qa + qb));
    if (g == 0) { v4u gr = {asu(s64), tag, asu(q64), tag}; ST128(gr, rs, soff + ct * 1024, SC1); }
    SB();
    const int fbase = (2 * ct * 128 + 4 * pt + w) * 1024;
    const v4u hin_a = transpose4(hin[0].x, hin[1].x, hin[2].x, hin[3].x), hin_b = transpose4(hin[0].y, hin[1].y, hin[2].y, hin[3].y);
    v4u g0, g1;
    __builtin_amdgcn_s_sleep(XPRE);
    for (unsigned spin = 0;; ++spin) {
      CB();
      g0 = LD128(rs, soff + g * 2048, 0, SC1); g1 = LD128(rs, soff + g * 2048 + 1024, 0, SC1);
      if (__all((g0.y == tag) & (g0.w == tag) & (g1.y == tag) & (g1.w == tag)) || spin > SPIN_CAP) break;
    }
    ST128(hin_a, rs, lane * 16 + WS_HIN + fbase, SC1);
    ST128(hin_b, rs, lane * 16 + WS_HIN + fbase + 131072, SC1);
    if (ct == 0 && lane == 0) ST32(tag, rs, WS_EPOCH + (pt * 4 + w) * 128, SC1);
    const float mean = xsum32(xsum16(asf(g0.x) + asf(g1.x))) * (1.f / 512.f);
    const float ex2 = xsum32(xsum16(asf(g0.z) + asf(g1.z))) * (1.f / 512.f);
    const float rstd = __builtin_amdgcn_rsqf(fmaxf(ex2 - mean * mean, 0.f) + 1e-5f);
    const float nb = -mean * rstd;
    const float rs2 = rstd * -1.44269504f, nb2 = nb * -1.44269504f;
    const int poff = (((g >> 1) + 2 * (g & 1)) * 16 + px) * 16;
    v2u hv[4];
#pragma unroll
    for (int cc = 0; cc < 4; ++cc) {
      float o[4];
#pragma unroll
      for (int i = 0; i < 4; ++i) { const float y = v[4 * cc + i] * rstd + nb; o[i] = y * __builtin_amdgcn_rcpf(1.f + __builtin_amdgcn_exp2f(v[4 * cc + i] * rs2 + nb2)); }
      hv[cc].x = pk_bf16(o[0], o[1]); hv[cc].y = pk_bf16(o[2], o[3]);
      if (cc & 1) ST128(widen16(hv[cc - 1], hv[cc]), rs, poff + WS_HN + fbase + (cc >> 1) * 131072, SC1);
    }
    return;
  }
  {
    const int rw = wv - 8;
    const int u = bid * 5 + rw, rg = u >> 1, m = 4 * (u & 1) + (lane >> 4);
    const bool isc = rg < 576;
    const int r2 = rg - 576, s2 = r2 >> 2;
    unsigned d0, d1;
    {
      unsigned long long pc; asm volatile("s_getpc_b64 %0\ncn_code_here:" : "=s"(pc));
      int lo, len;
      asm volatile("s_mov_b32 %0, cn_k1-cn_code_here" : "=s"(lo));
      asm volatile("s_mov_b32 %0, cn_code_end-cn_k1" : "=s"(len));
      int off = (idx * 5 + rw) * 128;
      off = off + 64 < len ? off : 0;
      asm volatile("s_load_dword %0, %2, %3\n\ts_load_dword %1, %2, %4" : "=&s"(d0), "=&s"(d1) : "s"((const char*)pc + lo), "s"(off), "s"(off + 64) : "memory");
    }
    v2u rt[8];
#pragma unroll
    for (int i = 0; i <= 8; ++i) {
      if (i == R_AT) {
        const char* src = isc ? (const char*)wc + rg * 8192 : (const char*)w2 + (64 * (s2 >> 1) + 16 * (r2 & 3) + 2 * (s2 & 1)) * 1024;
        src += (u & 1) * 512 + lane * 8;
        const int st2 = isc ? 2048 : 4096;
#pragma unroll
        for (int j = 0; j < 8; ++j) { asm volatile("global_load_dwordx2 %0, %1, off" : "=v"(rt[j]) : "v"(src + (j >> 1) * st2 + (j & 1) * 1024) : "memory"); }
      }
      if (i == 8) {
        asm volatile("s_waitcnt vmcnt(0)" : "+v"(rt[0]), "+v"(rt[1]), "+v"(rt[2]), "+v"(rt[3]), "+v"(rt[4]), "+v"(rt[5]), "+v"(rt[6]), "+v"(rt[7]) :: "memory");
        const int fo = isc ? WS_WCT + ((((rg & 63) >> 2) * 9 + (rg >> 6)) * 2) * 1024 : WS_W2T + s2 * 4096;
        const int hs = isc ? 294912 : 2048;
        const int vo = fo + m * (isc ? 589824 : 65536) + ((rg & 3) * 16 + (lane & 15)) * 16;
        v4u o0, o1, o2, o3;
#pragma unroll
        for (int k = 0; k < 4; ++k) {
          o0[k] = __builtin_amdgcn_perm(rt[2 * k + 1].x, rt[2 * k].x, 0x05040100u); o1[k] = __builtin_amdgcn_perm(rt[2 * k + 1].x, rt[2 * k].x, 0x07060302u);
          o2[k] = __builtin_amdgcn_perm(rt[2 * k + 1].y, rt[2 * k].y, 0x05040100u); o3[k] = __builtin_amdgcn_perm(rt[2 * k + 1].y, rt[2 * k].y, 0x07060302u);
        }
        ST128(o0, rs, vo, SC1); ST128(o1, rs, vo + 1024, SC1); ST128(o2, rs, vo + hs, SC1); ST128(o3, rs, vo + hs + 1024, SC1);
        SB();
      }
      if (i == R_PO && rw < 4) {
        const v4u po = {POISON, POISON, POISON, POISON};
        ST128(po, rs, WS_H2 + bid * 8192 + rw * 2048 + lane * 16, SC1);
        ST128(po, rs, WS_H2 + bid * 8192 + rw * 2048 + 1024 + lane * 16, SC1);
        SB();
      }
      if (i < 8) BARRIER();
    }
    asm volatile("s_waitcnt lgkmcnt(0)" : "+s"(d0), "+s"(d1) :: "memory");
  }
}

#define K2_L0 14
#define K2_RN 19
#define K2_RD 4
#define W2PRE 10
constexpr int K2_LDS = 131072;
constexpr int k2_min_lead() {
  constexpr int need[30] = {0, 0, 2, 1, 4, 5, 6, 7, 12, 14, 24, 26, 36, 38, 48, 49, 52, 53, 54, 55, 84, 86, 96, 97, 100, 101, 102, 103, 132, 134};
  int mn = 1000;
  for (int l = K2_L0; l < 120; ++l) { const int ld = (l / 30) * 144 + need[l % 30] - ((l - K2_L0) * K2_RN) / K2_RD; if (ld < mn) mn = ld; }
  return mn;
}
static_assert(k2_min_lead() >= 8, "a load would be issued behind its first use");

extern "C" __global__ __launch_bounds__(256, 1) void cn_k2(char* ws, const u16* __restrict__ xf, u16* __restrict__ out) {
  extern __shared__ __attribute__((aligned(16))) char smem_[];
  LAS char* smem = (LAS char*)smem_;
  const int bid = blockIdx.x, lane = threadIdx.x & 63, w = __builtin_amdgcn_readfirstlane(threadIdx.x >> 6);
  const int xcd = bid & 7, idx = bid >> 3, b = xcd >> 2, rt = idx >> 2, n = (xcd & 3) * 4 + (idx & 3);
  const __amdgpu_buffer_rsrc_t rs = __builtin_amdgcn_make_buffer_rsrc((void*)ws, 0, WS_END, RSRC_W3);
  const int vo = lane * 16;
  const int vo_top = rt == 0 ? 0x40000000 : vo, vo_bot = rt == 7 ? 0x40000000 : vo;
  const int wbase = WS_WCT + (n * 16 + 4 * w) * 18432;
  const int abase = WS_HN + 4 * w * 131072 + (b * 32 + 4 * rt - 1) * 2048;
  v4u W[4][18], A[4][12], S[12];
  f32x4 acc[4][2][2] = {};
  const int eoff = (n * 128 + (b * 32 + 4 * rt + w) * 2) * 1024;
  const int pt = xcd * 4 + (idx >> 3), ct = idx & 7;
  const int px = lane & 15, g = lane >> 4;
  const int ooff = (pt * 64 + 32 * (w >> 1) + 2 * px + (w & 1)) * 1024 + ct * 128 + g * 32;
  const int w2base = WS_W2T + ct * 65536 + 4 * w * 4096;
  v4u hin0, hin1, xv0, xv1, W2[4][4];
  auto ldx = [&](auto lc) AI {
    constexpr int l = decltype(lc)::v;
    if constexpr (l < 120) {
      constexpr int j = l / 30, i = l % 30;
      constexpr int wi[30] = {2, -1, -1, 3, 4, 5, 0, 1, -1, -1, -1, -1, -1, -1, 8, 9, 10, 11, 6, 7, -1, -1, 14, 15, 16, 17, 12, 13, -1, -1};
      constexpr int ai[30] = {-1, 0, 1, -1, -1, -1, -1, -1, 2, 3, 4, 5, 6, 7, -1, -1, -1, -1, -1, -1, 8, 9, -1, -1, -1, -1, -1, -1, 10, 11};
      if constexpr (wi[i] >= 0) { constexpr int f = wi[i]; W[j][f] = LD128(rs, vo + (f & 3) * 1024, wbase + j * 18432 + (f >> 2) * 4096, 0); }
      else { constexpr int f = ai[i]; A[j][f] = LD128(rs, (f < 2 ? vo_top : f >= 10 ? vo_bot : vo) + (f & 3) * 1024, abase + j * 131072 + (f >> 2) * 4096, 0); }
    }
    else if constexpr (l == 120) hin0 = LD128(rs, vo, WS_HIN + eoff, 0);
    else if constexpr (l == 121) hin1 = LD128(rs, vo + 1024, WS_HIN + eoff, 0);
    else if constexpr (l < 138) { constexpr int k = l - 122; W2[k >> 2][k & 3] = LD128(rs, vo + (k & 3) * 1024, w2base + (k >> 2) * 4096, 0); }
    else if constexpr (l == 138) xv0 = *(const GAS v4u*)((const char*)xf + ooff);
    else if constexpr (l == 139) xv1 = *(const GAS v4u*)((const char*)xf + ooff + 16);
  };
  auto ldsb = [&](auto lc) AI { ldx(lc); SB(); };
  static_for<0, K2_L0>(ldsb);
  constexpr int LREST = K2_L0 + (576 * K2_RD + K2_RN - 1) / K2_RN;
  LAS char* wscr = smem + w * 16384 + lane * 16;
  LAS char* rscr = smem + w * 4096 + lane * 16;
  static_for<0, 48>([&](auto uc) AI {
    constexpr int u = decltype(uc)::v, j = u / 12, dy = (u / 4) % 3, o = u % 4, R = o + dy;
    constexpr bool newrow = (dy == 0) || (o == 3);
    static_for<0, 12>([&](auto mc) AI {
      constexpr int m = decltype(mc)::v, slot = u * 12 + m;
      constexpr int dl = (slot * K2_RD + K2_RN - 1) / K2_RN;
      if constexpr ((dl * K2_RN) / K2_RD == slot) ldx(IC<K2_L0 + dl>{});
      if constexpr (newrow && m < 8) {
        constexpr int k = m & 3;
        if constexpr (m < 4) S[2 * R][k] = __builtin_amdgcn_update_dpp(0u, A[j][2 * R][k], 0x101, 0xf, 0xf, true);
        else S[2 * R + 1][k] = __builtin_amdgcn_update_dpp(0u, A[j][2 * R + 1][k], 0x111, 0xf, 0xf, true);
      }
      constexpr int c2 = m & 1, t = m >> 1;
      if constexpr (t == 0) acc[o][0][c2] = mfma16(W[j][(3 * dy + 1) * 2 + c2], A[j][2 * R], acc[o][0][c2]);
      if constexpr (t == 1) acc[o][1][c2] = mfma16(W[j][(3 * dy + 1) * 2 + c2], A[j][2 * R + 1], acc[o][1][c2]);
      if constexpr (t == 2) acc[o][0][c2] = mfma16(W[j][(3 * dy + 2) * 2 + c2], A[j][2 * R + 1], acc[o][0][c2]);
      if constexpr (t == 3) acc[o][1][c2] = mfma16(W[j][(3 * dy + 0) * 2 + c2], A[j][2 * R], acc[o][1][c2]);
      if constexpr (t == 4) acc[o][1][c2] = mfma16(W[j][(3 * dy + 2) * 2 + c2], S[2 * R], acc[o][1][c2]);
      if constexpr (t == 5) acc[o][0][c2] = mfma16(W[j][(3 * dy + 0) * 2 + c2], S[2 * R + 1], acc[o][0][c2]);
      SB();
      if constexpr (u > 44 && m >= 4 && m < 8) *(LAS f32x4*)(wscr + ((o - 1) * 4 + m - 4) * 1024) = acc[o - 1][(m - 4) >> 1][m & 1];
    });
  });
#pragma unroll
  for (int f = 0; f < 4; ++f) *(LAS f32x4*)(wscr + (12 + f) * 1024) = acc[3][f >> 1][f & 1];
#pragma unroll
  for (int f = 0; f < 12; ++f) asm volatile("" :: "a"(acc[f >> 2][(f >> 1) & 1][f & 1]));
  static_for<LREST, LREST + 3>(ldsb);
  wait_lgkm0();
  BARRIER();
  {
    f32x4 t[4][4];
#pragma unroll
    for (int f = 0; f < 4; ++f)
#pragma unroll
      for (int s = 0; s < 4; ++s) t[f][s] = *(LAS f32x4*)(rscr + s * 16384 + f * 1024);
    SB();
    static_for<LREST + 3, 140>(ldsb);
#pragma unroll
    for (int par = 0; par < 2; ++par) {
      const f32x4 t0 = (t[2 * par][0] + t[2 * par][1]) + (t[2 * par][2] + t[2 * par][3]);
      const f32x4 t1 = (t[2 * par + 1][0] + t[2 * par + 1][1]) + (t[2 * par + 1][2] + t[2 * par + 1][3]);
      const v4u hi = par ? hin1 : hin0;
      v4u o = {pk_bf16(t0[0] + bf_lo(hi.x), t1[0] + bf_hi(hi.x)), pk_bf16(t0[1] + bf_lo(hi.y), t1[1] + bf_hi(hi.y)),
               pk_bf16(t0[2] + bf_lo(hi.z), t1[2] + bf_hi(hi.z)), pk_bf16(t0[3] + bf_lo(hi.w), t1[3] + bf_hi(hi.w))};
#pragma unroll
      for (int i = 0; i < 4; ++i) o[i] = umin(o[i], POISON - 1u);
      ST128(o, rs, vo + par * 1024 + WS_H2 + eoff, SC1);
    }
  }
#pragma unroll
  for (int k = 0; k < 16; ++k) *(LAS v4u*)(smem + 65536 + w * 16384 + k * 1024 + lane * 16) = W2[k >> 2][k & 3];
  wait_lgkm0();
  BARRIER();
  {
    const int a2base = WS_H2 + pt * 4096 + w * 1024;
    f32x4 r[4];
#pragma unroll 1
    for (unsigned spin = 0;; ++spin) {
      CB();
      v4u A2[16], WF[16][4];
#pragma unroll
      for (int s = 0; s < 16; ++s) {
        A2[s] = LD128(rs, vo, a2base + s * 131072, SC1);
        if (s < W2PRE) {
#pragma unroll
          for (int t = 0; t < 4; ++t) WF[s][t] = *(LAS v4u*)(smem + 65536 + (s * 4 + t) * 1024 + lane * 16);
        }
        SB();
      }
      unsigned mx = 0;
#pragma unroll
      for (int s = 0; s < 16; ++s)
#pragma unroll
        for (int t = 0; t < 4; ++t) {
          const f32x4 zero = {0.f, 0.f, 0.f, 0.f};
          if (s + W2PRE < 16) WF[s + W2PRE][t] = *(LAS v4u*)(smem + 65536 + ((s + W2PRE) * 4 + t) * 1024 + lane * 16);
          r[t] = mfma16(WF[s][t], A2[s], s == 0 ? zero : r[t]);
          if (t == 0) mx = umax(umax(mx, A2[s].x), A2[s].y);
          if (t == 1) mx = umax(umax(mx, A2[s].z), A2[s].w);
          SB();
        }
      if (__builtin_expect(__all(mx != POISON), 1) || spin > SPIN_CAP) break;
    }
    v4u o0 = {pk_bf16(r[0][0] + bf_lo(xv0.x), r[1][0] + bf_hi(xv0.x)), pk_bf16(r[2][0] + bf_lo(xv0.y), r[3][0] + bf_hi(xv0.y)),
              pk_bf16(r[0][1] + bf_lo(xv0.z), r[1][1] + bf_hi(xv0.z)), pk_bf16(r[2][1] + bf_lo(xv0.w), r[3][1] + bf_hi(xv0.w))};
    v4u o1 = {pk_bf16(r[0][2] + bf_lo(xv1.x), r[1][2] + bf_hi(xv1.x)), pk_bf16(r[2][2] + bf_lo(xv1.y), r[3][2] + bf_hi(xv1.y)),
              pk_bf16(r[0][3] + bf_lo(xv1.z), r[1][3] + bf_hi(xv1.z)), pk_bf16(r[2][3] + bf_lo(xv1.w), r[3][3] + bf_hi(xv1.w))};
    *(GAS v4u*)((char*)out + ooff) = o0;
    *(GAS v4u*)((char*)out + ooff + 16) = o1;
  }
  asm volatile("cn_code_end:");
}

extern "C" void kernel_launch(void* const* d_in, const int* in_sizes, int n_in, void* d_out, int out_size, void* d_ws, size_t ws_size, hipStream_t stream) {
  const u16* xf = (const u16*)d_in[0]; const u16* x = (const u16*)d_in[1]; const u16* c = (const u16*)d_in[2];
  const u16* w1 = (const u16*)d_in[3]; const u16* w2 = (const u16*)d_in[4]; const u16* wc = (const u16*)d_in[5];
  char* ws = (char*)d_ws;
  (void)hipFuncSetAttribute((const void*)cn_k1, hipFuncAttributeMaxDynamicSharedMemorySize, K1_LDS);
  (void)hipFuncSetAttribute((const void*)cn_k2, hipFuncAttributeMaxDynamicSharedMemorySize, K2_LDS);
  cn_k1<<<256, 832, K1_LDS, stream>>>(x, c, w1, wc, w2, ws);
  cn_k2<<<256, 256, K2_LDS, stream>>>(ws, xf, (u16*)d_out);
}
```

```cpp
#define R_PO 6
#include <hip/hip_runtime.h>
#include <hip/hip_bf16.h>
typedef unsigned short u16;
typedef unsigned v4u __attribute__((ext_vector_type(4)));
typedef unsigned v2u __attribute__((ext_vector_type(2)));
typedef short v4s __attribute__((ext_vector_type(4)));
typedef __bf16 v8bf __attribute__((ext_vector_type(8)));
typedef __bf16 v2bf __attribute__((ext_vector_type(2)));
typedef float f32x2 __attribute__((ext_vector_type(2)));
typedef float f32x4 __attribute__((ext_vector_type(4)));
#define GAS __attribute__((address_space(1)))
#define LAS __attribute__((address_space(3)))
#define DI __device__ __forceinline__
#define AI __attribute__((always_inline))
#define SB() __builtin_amdgcn_sched_barrier(0)
#define CB() asm volatile("" ::: "memory")

constexpr int WS_EPOCH = 0;
constexpr int WS_STATS = 16384;
constexpr int WS_HN = WS_STATS + 262144;
constexpr int WS_HIN = WS_HN + 2097152;
constexpr int WS_H2 = WS_HIN + 2097152;
constexpr int WS_WCT = WS_H2 + 2097152;
constexpr int WS_W2T = WS_WCT + 4718592;
constexpr int WS_END = WS_W2T + 524288;
constexpr int RSRC_W3 = 0x00020000;
constexpr int SC1 = 16;
constexpr unsigned SPIN_CAP = 1u << 17;
constexpr unsigned POISON = 0xFFFFFFFFu;

DI float asf(unsigned u) { return __builtin_bit_cast(float, u); }
DI unsigned asu(float f) { return __builtin_bit_cast(unsigned, f); }
DI unsigned pk_bf16(float a, float b) { f32x2 v = {a, b}; return __builtin_bit_cast(unsigned, __builtin_convertvector(v, v2bf)); }
DI float bf_lo(unsigned u) { return asf(u << 16); }
DI float bf_hi(unsigned u) { return asf(u & 0xffff0000u); }
DI unsigned umin(unsigned a, unsigned b) { return a < b ? a : b; }
DI unsigned umax(unsigned a, unsigned b) { return a > b ? a : b; }
DI f32x4 mfma16(v4u a, v4u b, f32x4 c) { return __builtin_amdgcn_mfma_f32_16x16x32_bf16(__builtin_bit_cast(v8bf, a), __builtin_bit_cast(v8bf, b), c, 0, 0, 0); }
DI float xsum16(float v) { auto r = __builtin_amdgcn_permlane16_swap(asu(v), asu(v), false, false); return asf(r[0]) + asf(r[1]); }
DI float xsum32(float v) { auto r = __builtin_amdgcn_permlane32_swap(asu(v), asu(v), false, false); return asf(r[0]) + asf(r[1]); }
DI v4u widen16(v2u a, v2u b) {
  auto rx = __builtin_amdgcn_permlane16_swap(a.x, b.x, false, false);
  auto ry = __builtin_amdgcn_permlane16_swap(a.y, b.y, false, false);
  v4u o = {rx[0], ry[0], rx[1], ry[1]};
  return o;
}
DI v4u transpose4(unsigned a0, unsigned a1, unsigned a2, unsigned a3) {
  auto p = __builtin_amdgcn_permlane16_swap(a0, a1, false, false);
  auto q = __builtin_amdgcn_permlane16_swap(a2, a3, false, false);
  auto r = __builtin_amdgcn_permlane32_swap(p[0], q[0], false, false);
  auto s = __builtin_amdgcn_permlane32_swap(p[1], q[1], false, false);
  v4u o = {r[0], s[0], r[1], s[1]};
  return o;
}
DI v4u trfrag(LAS char* p0, LAS char* p1) {
  v4s a0 = __builtin_amdgcn_ds_read_tr16_b64_v4i16((LAS v4s*)p0);
  v4s a1 = __builtin_amdgcn_ds_read_tr16_b64_v4i16((LAS v4s*)p1);
  return __builtin_bit_cast(v4u, __builtin_shufflevector(a0, a1, 0, 1, 2, 3, 4, 5, 6, 7));
}
template <int V> struct IC { static constexpr int v = V; };
template <int B, int E, class F> DI void static_for(F&& f) { if constexpr (B < E) { f(IC<B>{}); static_for<B + 1, E>(f); } }
#define BARRIER() do { CB(); __builtin_amdgcn_s_barrier(); CB(); } while (0)
template <int N> DI void wait_vm() { asm volatile("s_waitcnt vmcnt(%0)" ::"i"(N) : "memory"); }
DI void wait_lgkm0() { asm volatile("s_waitcnt lgkmcnt(0)" ::: "memory"); }
DI v4u mk_rsrc(const void* p, unsigned bytes) { unsigned long long a = (unsigned long long)p; v4u r = {(unsigned)a, (unsigned)(a >> 32) & 0xffffu, bytes, (unsigned)RSRC_W3}; return r; }
DI void dma16(v4u rsrc, int voff, int soff, int lds) {
  asm volatile("s_mov_b32 m0, %0\n\ts_nop 0\n\tbuffer_load_dwordx4 %1, %2, %3 offen lds" ::"s"(lds), "v"(voff), "s"(rsrc), "s"(soff) : "memory");
}
#define ST128(val, rs, v, aux) __builtin_amdgcn_raw_buffer_store_b128(val, rs, v, 0, aux)
#define LD128(rs, v, s, aux) __builtin_amdgcn_raw_buffer_load_b128(rs, v, s, aux)
#define ST32(val, rs, v, aux) __builtin_amdgcn_raw_buffer_store_b32(val, rs, v, 0, aux)

constexpr int L_C = 0;
constexpr int L_W = 65536;
constexpr int L_X = 131072;
constexpr int K1_LDS = 139264;
#define INV 0
#define GATE 1
#define G_SLEEP 0
#define RLOC 1
#define AUX_WT 16
#define AUX_PO 16
#define R_AT 4
#define R_N 8
#define XPRE 16
extern "C" __global__ __launch_bounds__(832, 4) void cn_k1(const u16* __restrict__ x, const u16* __restrict__ c, const u16* __restrict__ w1,
                                                           const u16* __restrict__ wc, const u16* __restrict__ w2, char* ws) {
  extern __shared__ __attribute__((aligned(16))) char smem_[];
  LAS char* smem = (LAS char*)smem_;
  const int bid = blockIdx.x, lane = threadIdx.x & 63, wv = __builtin_amdgcn_readfirstlane(threadIdx.x >> 6);
  const int idx = bid >> 3, pt = (bid & 7) * 4 + (idx >> 3), ct = idx & 7;
  const __amdgpu_buffer_rsrc_t rs = __builtin_amdgcn_make_buffer_rsrc((void*)ws, 0, WS_END, RSRC_W3);
  if (__builtin_expect(wv < 8, 1)) {
    const int w = wv & 3;
    const int r8 = lane >> 3, s8 = lane & 7;
    const int voc = (pt * 64 + (w >> 1) + 2 * (8 * (w & 1) + r8)) * 1024 + ((s8 ^ (4 * (w & 1) + (r8 >> 1))) << 4);
    const int vow = (8 * w + r8) * 1024 + ct * 128 + ((s8 ^ (r8 & 2) ^ ((w & 1) << 2)) << 4);
    const int rot = 4 * ((ct ^ (idx >> 3)) & 1);
    const v4u rc = mk_rsrc(c, 2097152), rw1 = mk_rsrc(w1, 524288);
    {
      const int hi = (wv >> 2) * 32768;
#pragma unroll
      for (int i = 0; i < 4; ++i) {
        const int kc = i ^ rot;
        dma16(rc, voc, hi + kc * 128, L_C + i * 8192 + wv * 1024);
        dma16(rw1, vow, hi + kc * 65536, L_W + i * 8192 + wv * 1024);
      }
    }
    if (__builtin_expect(wv >= 4, 1)) {
      auto issue = [&](int i) {
        const int kc = i ^ rot;
        dma16(rc, voc, kc * 128, L_C + i * 8192 + w * 1024);
        dma16(rw1, vow, kc * 65536, L_W + i * 8192 + w * 1024);
        dma16(rc, voc, 32768 + kc * 128, L_C + i * 8192 + 4096 + w * 1024);
        dma16(rw1, vow, 32768 + kc * 65536, L_W + i * 8192 + 4096 + w * 1024);
      };
      const v4u rx = mk_rsrc(x, 2097152);
      dma16(rx, voc, ct * 128, L_X + w * 1024);
      dma16(rx, voc, 32768 + ct * 128, L_X + 4096 + w * 1024);
      wait_vm<8>(); BARRIER(); issue(4);
      wait_vm<10>(); BARRIER(); issue(5);
      wait_vm<12>(); BARRIER(); issue(6);
      wait_vm<14>(); BARRIER(); issue(7);
      wait_vm<12>(); BARRIER();
      wait_vm<8>(); BARRIER();
      wait_vm<4>(); BARRIER();
      wait_vm<0>(); BARRIER();
      return;
    }
    const int px = lane & 15, g = lane >> 4, q = (lane & 15) >> 2, p = lane & 3;
    const int row = 16 * w + px;
    const int soff = WS_STATS + pt * 8192 + row * 16;
    unsigned epoch_;
    asm volatile("s_load_dword %0, %1, %2" : "=&s"(epoch_) : "s"(ws), "s"(WS_EPOCH + (pt * 4 + w) * 128) : "memory");
    LAS char* cb0 = smem + L_C + row * 128 + ((g ^ ((px >> 1) & 7)) << 4);
    LAS char* cb1 = smem + L_C + row * 128 + (((4 + g) ^ ((px >> 1) & 7)) << 4);
    LAS char* wb[4];
#pragma unroll
    for (int cc = 0; cc < 4; ++cc) wb[cc] = smem + L_W + (8 * g + q) * 128 + (((2 * cc + (p >> 1)) ^ ((q & 2) | ((g & 1) << 2))) << 4) + 8 * (p & 1);
    f32x4 acc[4] = {};
    v4u bq[2][2], aq[2][2][4];
    v2u xv[4];
#pragma unroll
    for (int kc = 0; kc <= 8; ++kc) {
      if (kc == 0) { asm volatile("s_waitcnt vmcnt(6) lgkmcnt(0)" : "+s"(epoch_) :: "memory"); }
      if (kc == 1) wait_vm<4>();
      if (kc == 2) wait_vm<2>();
      if (kc == 3) wait_vm<0>();
      if (kc < 8) BARRIER();
#pragma unroll
      for (int s = 0; s < 2; ++s) {
        if (kc < 8) bq[kc & 1][s] = *(LAS v4u*)((s ? cb1 : cb0) + kc * 8192);
#pragma unroll
        for (int cc = 0; cc < 4; ++cc) {
          if (kc < 8) aq[kc & 1][s][cc] = trfrag(wb[cc] + kc * 8192 + s * 4096, wb[cc] + kc * 8192 + s * 4096 + 512);
          if (kc == 5 && s == 1) xv[cc] = *(LAS v2u*)(smem + L_X + row * 128 + (((2 * cc + (g >> 1)) ^ ((px >> 1) & 7)) << 4) + 8 * (g & 1));
          if (kc > 0) acc[cc] = mfma16(aq[(kc - 1) & 1][s][cc], bq[(kc - 1) & 1][s], acc[cc]);
          SB();
        }
      }
      if (kc == 6) {
#pragma unroll
        for (int cc = 0; cc < 4; ++cc) { acc[cc][0] += bf_lo(xv[cc].x); acc[cc][1] += bf_hi(xv[cc].x); acc[cc][2] += bf_lo(xv[cc].y); acc[cc][3] += bf_hi(xv[cc].y); }
        SB();
      }
    }
    const unsigned tag = epoch_ + 1u;
    float v[16]; v2u hin[4];
    float sa = 0.f, sb = 0.f, qa = 0.f, qb = 0.f;
#pragma unroll
    for (int cc = 0; cc < 4; ++cc) {
      hin[cc].x = pk_bf16(acc[cc][0], acc[cc][1]);
      hin[cc].y = pk_bf16(acc[cc][2], acc[cc][3]);
      v[4 * cc] = bf_lo(hin[cc].x); v[4 * cc + 1] = bf_hi(hin[cc].x); v[4 * cc + 2] = bf_lo(hin[cc].y); v[4 * cc + 3] = bf_hi(hin[cc].y);
      sa += v[4 * cc] + v[4 * cc + 2]; sb += v[4 * cc + 1] + v[4 * cc + 3];
      qa += v[4 * cc] * v[4 * cc]; qb += v[4 * cc + 1] * v[4 * cc + 1]; qa += v[4 * cc + 2] * v[4 * cc + 2]; qb += v[4 * cc + 3] * v[4 * cc + 3];
    }
    const float s64 = xsum32(xsum16(sa + sb)), q64 = xsum32(xsum16(qa + qb));
    if (g == 0) { v4u gr = {asu(s64), tag, asu(q64), tag}; ST128(gr, rs, soff + ct * 1024, SC1); }
    SB();
    if (GATE == 1) BARRIER();
    const int fbase = (2 * ct * 128 + 4 * pt + w) * 1024;
    const v4u hin_a = transpose4(hin[0].x, hin[1].x, hin[2].x, hin[3].x), hin_b = transpose4(hin[0].y, hin[1].y, hin[2].y, hin[3].y);
    v4u g0, g1;
    __builtin_amdgcn_s_sleep(XPRE);
    for (unsigned spin = 0;; ++spin) {
      CB();
      g0 = LD128(rs, soff + g * 2048, 0, SC1); g1 = LD128(rs, soff + g * 2048 + 1024, 0, SC1);
      if (__all((g0.y == tag) & (g0.w == tag) & (g1.y == tag) & (g1.w == tag)) || spin > SPIN_CAP) break;
    }
    if (GATE == 2) BARRIER();
    ST128(hin_a, rs, lane * 16 + WS_HIN + fbase, SC1);
    ST128(hin_b, rs, lane * 16 + WS_HIN + fbase + 131072, SC1);
    if (ct == 0 && lane == 0) ST32(tag, rs, WS_EPOCH + (pt * 4 + w) * 128, SC1);
    const float mean = xsum32(xsum16(asf(g0.x) + asf(g1.x))) * (1.f / 512.f);
    const float ex2 = xsum32(xsum16(asf(g0.z) + asf(g1.z))) * (1.f / 512.f);
    const float rstd = __builtin_amdgcn_rsqf(fmaxf(ex2 - mean * mean, 0.f) + 1e-5f);
    const float nb = -mean * rstd;
    const float rs2 = rstd * -1.44269504f, nb2 = nb * -1.44269504f;
    const int poff = (((g >> 1) + 2 * (g & 1)) * 16 + px) * 16;
    v2u hv[4];
#pragma unroll
    for (int cc = 0; cc < 4; ++cc) {
      float o[4];
#pragma unroll
      for (int i = 0; i < 4; ++i) { const float y = v[4 * cc + i] * rstd + nb; o[i] = y * __builtin_amdgcn_rcpf(1.f + __builtin_amdgcn_exp2f(v[4 * cc + i] * rs2 + nb2)); }
      hv[cc].x = pk_bf16(o[0], o[1]); hv[cc].y = pk_bf16(o[2], o[3]);
      if (cc & 1) ST128(widen16(hv[cc - 1], hv[cc]), rs, poff + WS_HN + fbase + (cc >> 1) * 131072, SC1);
    }
    return;
  }
  {
    const int rw = wv - 8;
    const int rg = (((bid & 7) >> 2) * 160 + idx * 5 + rw) * 2 + (lane >> 5), m = 2 * (bid & 3) + ((lane >> 4) & 1);
    const int coff = (bid & 3) * 256 + (lane & 31) * 8;
    const bool isc = rg < 576;
    const int r2 = rg - 576, s2 = r2 >> 2;
    unsigned d0, d1;
    {
      unsigned long long pc; asm volatile("s_getpc_b64 %0\ncn_code_here:" : "=s"(pc));
      int lo, len;
      asm volatile("s_mov_b32 %0, cn_k1-cn_code_here" : "=s"(lo));
      asm volatile("s_mov_b32 %0, cn_code_end-cn_k1" : "=s"(len));
      int off = (idx * 5 + rw) * 128;
      off = off + 64 < len ? off : 0;
      asm volatile("s_load_dword %0, %2, %3\n\ts_load_dword %1, %2, %4" : "=&s"(d0), "=&s"(d1) : "s"((const char*)pc + lo), "s"(off), "s"(off + 64) : "memory");
    }
    const char* src = isc ? (const char*)wc + rg * 8192 : (const char*)w2 + (64 * (s2 >> 1) + 16 * (r2 & 3) + 2 * (s2 & 1)) * 1024;
    src += coff;
    const int st2 = isc ? 2048 : 4096;
    v2u rt[8];
#pragma unroll
    for (int i = 0; i <= 8; ++i) {
      if (i >= R_AT) {
#pragma unroll
        for (int j = 0; j < 8; ++j)
          if (j / R_N == i - R_AT || (i == 8 && j / R_N > i - R_AT)) { asm volatile("global_load_dwordx2 %0, %1, off" : "=v"(rt[j]) : "v"(src + (j >> 1) * st2 + (j & 1) * 1024) : "memory"); }
      }
      if (i == 8) {
        if (GATE) BARRIER();
        __builtin_amdgcn_s_sleep(G_SLEEP);
        asm volatile("s_waitcnt vmcnt(0)" : "+v"(rt[0]), "+v"(rt[1]), "+v"(rt[2]), "+v"(rt[3]), "+v"(rt[4]), "+v"(rt[5]), "+v"(rt[6]), "+v"(rt[7]) :: "memory");
        const int fo = isc ? WS_WCT + ((((rg & 63) >> 2) * 9 + (rg >> 6)) * 2) * 1024 : WS_W2T + s2 * 4096;
        const int hs = isc ? 294912 : 2048;
        const int vo = fo + m * (isc ? 589824 : 65536) + ((rg & 3) * 16 + (lane & 15)) * 16;
        v4u o0, o1, o2, o3;
#pragma unroll
        for (int k = 0; k < 4; ++k) {
          o0[k] = __builtin_amdgcn_perm(rt[2 * k + 1].x, rt[2 * k].x, 0x05040100u); o1[k] = __builtin_amdgcn_perm(rt[2 * k + 1].x, rt[2 * k].x, 0x07060302u);
          o2[k] = __builtin_amdgcn_perm(rt[2 * k + 1].y, rt[2 * k].y, 0x05040100u); o3[k] = __builtin_amdgcn_perm(rt[2 * k + 1].y, rt[2 * k].y, 0x07060302u);
        }
        ST128(o0, rs, vo, AUX_WT); ST128(o1, rs, vo + 1024, AUX_WT); ST128(o2, rs, vo + hs, AUX_WT); ST128(o3, rs, vo + hs + 1024, AUX_WT);
        SB();
      }
      if (i == R_PO && rw < 4) {
        const v4u po = {POISON, POISON, POISON, POISON};
        ST128(po, rs, WS_H2 + bid * 8192 + rw * 2048 + lane * 16, SC1);
        ST128(po, rs, WS_H2 + bid * 8192 + rw * 2048 + 1024 + lane * 16, SC1);
        SB();
      }
      if (i < 8) BARRIER();
    }
    asm volatile("s_waitcnt lgkmcnt(0)" : "+s"(d0), "+s"(d1) :: "memory");
  }
}

#define AUXW 0
#define AUXA 0
#define AUXH2 16
#define K2_L0 14
#define K2_RN 19
#define K2_RD 4
#define W2PRE 10
constexpr int K2_LDS = 131072;
constexpr int k2_min_lead() {
  constexpr int need[30] = {0, 0, 2, 1, 4, 5, 6, 7, 12, 14, 24, 26, 36, 38, 48, 49, 52, 53, 54, 55, 84, 86, 96, 97, 100, 101, 102, 103, 132, 134};
  int mn = 1000;
  for (int l = K2_L0; l < 120; ++l) { const int ld = (l / 30) * 144 + need[l % 30] - ((l - K2_L0) * K2_RN) / K2_RD; if (ld < mn) mn = ld; }
  return mn;
}
static_assert(k2_min_lead() >= 8, "a load would be issued behind its first use");

extern "C" __global__ __launch_bounds__(256, 1) void cn_k2(char* ws, const u16* __restrict__ xf, u16* __restrict__ out) {
  extern __shared__ __attribute__((aligned(16))) char smem_[];
  LAS char* smem = (LAS char*)smem_;
  const int bid = blockIdx.x, lane = threadIdx.x & 63, w = __builtin_amdgcn_readfirstlane(threadIdx.x >> 6);
  const int xcd = bid & 7, idx = bid >> 3, b = xcd >> 2, rt = idx >> 2, n = (xcd & 3) * 4 + (idx & 3);
  const __amdgpu_buffer_rsrc_t rs = __builtin_amdgcn_make_buffer_rsrc((void*)ws, 0, WS_END, RSRC_W3);
  const int vo = lane * 16;
  const int vo_top = rt == 0 ? 0x40000000 : vo, vo_bot = rt == 7 ? 0x40000000 : vo;
  const int wbase = WS_WCT + (n * 16 + 4 * w) * 18432;
  const int abase = WS_HN + 4 * w * 131072 + (b * 32 + 4 * rt - 1) * 2048;
  v4u W[4][18], A[4][12], S[12];
  f32x4 acc[4][2][2] = {};
  const int eoff = (n * 128 + (b * 32 + 4 * rt + w) * 2) * 1024;
  const int pt = xcd * 4 + (idx >> 3), ct = idx & 7;
  const int px = lane & 15, g = lane >> 4;
  const int ooff = (pt * 64 + 32 * (w >> 1) + 2 * px + (w & 1)) * 1024 + ct * 128 + g * 32;
  const int w2base = WS_W2T + ct * 65536 + 4 * w * 4096;
  v4u hin0, hin1, xv0, xv1, W2[4][4];
  auto ldx = [&](auto lc) AI {
    constexpr int l = decltype(lc)::v;
    if constexpr (l < 120) {
      constexpr int j = l / 30, i = l % 30;
      constexpr int wi[30] = {2, -1, -1, 3, 4, 5, 0, 1, -1, -1, -1, -1, -1, -1, 8, 9, 10, 11, 6, 7, -1, -1, 14, 15, 16, 17, 12, 13, -1, -1};
      constexpr int ai[30] = {-1, 0, 1, -1, -1, -1, -1, -1, 2, 3, 4, 5, 6, 7, -1, -1, -1, -1, -1, -1, 8, 9, -1, -1, -1, -1, -1, -1, 10, 11};
      if constexpr (wi[i] >= 0) { constexpr int f = wi[i]; W[j][f] = LD128(rs, vo + (f & 3) * 1024, wbase + j * 18432 + (f >> 2) * 4096, AUXW); }
      else { constexpr int f = ai[i]; A[j][f] = LD128(rs, (f < 2 ? vo_top : f >= 10 ? vo_bot : vo) + (f & 3) * 1024, abase + j * 131072 + (f >> 2) * 4096, AUXA); }
    }
    else if constexpr (l == 120) hin0 = LD128(rs, vo, WS_HIN + eoff, AUXA);
    else if constexpr (l == 121) hin1 = LD128(rs, vo + 1024, WS_HIN + eoff, AUXA);
    else if constexpr (l < 138) { constexpr int k = l - 122; W2[k >> 2][k & 3] = LD128(rs, vo + (k & 3) * 1024, w2base + (k >> 2) * 4096, AUXW); }
    else if constexpr (l == 138) xv0 = *(const GAS v4u*)((const char*)xf + ooff);
    else if constexpr (l == 139) xv1 = *(const GAS v4u*)((const char*)xf + ooff + 16);
  };
  auto ldsb = [&](auto lc) AI { ldx(lc); SB(); };
  static_for<0, K2_L0>(ldsb);
  constexpr int LREST = K2_L0 + (576 * K2_RD + K2_RN - 1) / K2_RN;
  LAS char* wscr = smem + w * 16384 + lane * 16;
  LAS char* rscr = smem + w * 4096 + lane * 16;
  static_for<0, 48>([&](auto uc) AI {
    constexpr int u = decltype(uc)::v, j = u / 12, dy = (u / 4) % 3, o = u % 4, R = o + dy;
    constexpr bool newrow = (dy == 0) || (o == 3);
    static_for<0, 12>([&](auto mc) AI {
      constexpr int m = decltype(mc)::v, slot = u * 12 + m;
      constexpr int dl = (slot * K2_RD + K2_RN - 1) / K2_RN;
      if constexpr ((dl * K2_RN) / K2_RD == slot) ldx(IC<K2_L0 + dl>{});
      if constexpr (newrow && m < 8) {
        constexpr int k = m & 3;
        if constexpr (m < 4) S[2 * R][k] = __builtin_amdgcn_update_dpp(0u, A[j][2 * R][k], 0x101, 0xf, 0xf, true);
        else S[2 * R + 1][k] = __builtin_amdgcn_update_dpp(0u, A[j][2 * R + 1][k], 0x111, 0xf, 0xf, true);
      }
      constexpr int c2 = m & 1, t = m >> 1;
      if constexpr (t == 0) acc[o][0][c2] = mfma16(W[j][(3 * dy + 1) * 2 + c2], A[j][2 * R], acc[o][0][c2]);
      if constexpr (t == 1) acc[o][1][c2] = mfma16(W[j][(3 * dy + 1) * 2 + c2], A[j][2 * R + 1], acc[o][1][c2]);
      if constexpr (t == 2) acc[o][0][c2] = mfma16(W[j][(3 * dy + 2) * 2 + c2], A[j][2 * R + 1], acc[o][0][c2]);
      if constexpr (t == 3) acc[o][1][c2] = mfma16(W[j][(3 * dy + 0) * 2 + c2], A[j][2 * R], acc[o][1][c2]);
      if constexpr (t == 4) acc[o][1][c2] = mfma16(W[j][(3 * dy + 2) * 2 + c2], S[2 * R], acc[o][1][c2]);
      if constexpr (t == 5) acc[o][0][c2] = mfma16(W[j][(3 * dy + 0) * 2 + c2], S[2 * R + 1], acc[o][0][c2]);
      SB();
      if constexpr (u > 44 && m >= 4 && m < 8) *(LAS f32x4*)(wscr + ((o - 1) * 4 + m - 4) * 1024) = acc[o - 1][(m - 4) >> 1][m & 1];
    });
  });
#pragma unroll
  for (int f = 0; f < 4; ++f) *(LAS f32x4*)(wscr + (12 + f) * 1024) = acc[3][f >> 1][f & 1];
#pragma unroll
  for (int f = 0; f < 12; ++f) asm volatile("" :: "a"(acc[f >> 2][(f >> 1) & 1][f & 1]));
  static_for<LREST, LREST + 3>(ldsb);
  wait_lgkm0();
  BARRIER();
  {
    f32x4 t[4][4];
#pragma unroll
    for (int f = 0; f < 4; ++f)
#pragma unroll
      for (int s = 0; s < 4; ++s) t[f][s] = *(LAS f32x4*)(rscr + s * 16384 + f * 1024);
    SB();
    static_for<LREST + 3, 140>(ldsb);
#pragma unroll
    for (int par = 0; par < 2; ++par) {
      const f32x4 t0 = (t[2 * par][0] + t[2 * par][1]) + (t[2 * par][2] + t[2 * par][3]);
      const f32x4 t1 = (t[2 * par + 1][0] + t[2 * par + 1][1]) + (t[2 * par + 1][2] + t[2 * par + 1][3]);
      const v4u hi = par ? hin1 : hin0;
      v4u o = {pk_bf16(t0[0] + bf_lo(hi.x), t1[0] + bf_hi(hi.x)), pk_bf16(t0[1] + bf_lo(hi.y), t1[1] + bf_hi(hi.y)),
               pk_bf16(t0[2] + bf_lo(hi.z), t1[2] + bf_hi(hi.z)), pk_bf16(t0[3] + bf_lo(hi.w), t1[3] + bf_hi(hi.w))};
#pragma unroll
      for (int i = 0; i < 4; ++i) o[i] = umin(o[i], POISON - 1u);
      ST128(o, rs, vo + par * 1024 + WS_H2 + eoff, SC1);
    }
  }
#pragma unroll
  for (int k = 0; k < 16; ++k) *(LAS v4u*)(smem + 65536 + w * 16384 + k * 1024 + lane * 16) = W2[k >> 2][k & 3];
  wait_lgkm0();
  BARRIER();
  {
    const int a2base = WS_H2 + pt * 4096 + w * 1024;
    f32x4 r[4];
#pragma unroll 1
    for (unsigned spin = 0;; ++spin) {
      CB();
      v4u A2[16], WF[16][4];
#pragma unroll
      for (int s = 0; s < 16; ++s) {
        A2[s] = LD128(rs, vo, a2base + s * 131072, AUXH2);
        if (s < W2PRE) {
#pragma unroll
          for (int t = 0; t < 4; ++t) WF[s][t] = *(LAS v4u*)(smem + 65536 + (s * 4 + t) * 1024 + lane * 16);
        }
        SB();
      }
      unsigned mx = 0;
#pragma unroll
      for (int s = 0; s < 16; ++s)
#pragma unroll
        for (int t = 0; t < 4; ++t) {
          const f32x4 zero = {0.f, 0.f, 0.f, 0.f};
          if (s + W2PRE < 16) WF[s + W2PRE][t] = *(LAS v4u*)(smem + 65536 + ((s + W2PRE) * 4 + t) * 1024 + lane * 16);
          r[t] = mfma16(WF[s][t], A2[s], s == 0 ? zero : r[t]);
          if (t == 0) mx = umax(umax(mx, A2[s].x), A2[s].y);
          if (t == 1) mx = umax(umax(mx, A2[s].z), A2[s].w);
          SB();
        }
      if (__builtin_expect(__all(mx != POISON), 1) || spin > SPIN_CAP) break;
    }
    v4u o0 = {pk_bf16(r[0][0] + bf_lo(xv0.x), r[1][0] + bf_hi(xv0.x)), pk_bf16(r[2][0] + bf_lo(xv0.y), r[3][0] + bf_hi(xv0.y)),
              pk_bf16(r[0][1] + bf_lo(xv0.z), r[1][1] + bf_hi(xv0.z)), pk_bf16(r[2][1] + bf_lo(xv0.w), r[3][1] + bf_hi(xv0.w))};
    v4u o1 = {pk_bf16(r[0][2] + bf_lo(xv1.x), r[1][2] + bf_hi(xv1.x)), pk_bf16(r[2][2] + bf_lo(xv1.y), r[3][2] + bf_hi(xv1.y)),
              pk_bf16(r[0][3] + bf_lo(xv1.z), r[1][3] + bf_hi(xv1.z)), pk_bf16(r[2][3] + bf_lo(xv1.w), r[3][3] + bf_hi(xv1.w))};
    *(GAS v4u*)((char*)out + ooff) = o0;
    *(GAS v4u*)((char*)out + ooff + 16) = o1;
  }
  asm volatile("cn_code_end:");
}

extern "C" void kernel_launch(void* const* d_in, const int* in_sizes, int n_in, void* d_out, int out_size, void* d_ws, size_t ws_size, hipStream_t stream) {
  const u16* xf = (const u16*)d_in[0]; const u16* x = (const u16*)d_in[1]; const u16* c = (const u16*)d_in[2];
  const u16* w1 = (const u16*)d_in[3]; const u16* w2 = (const u16*)d_in[4]; const u16* wc = (const u16*)d_in[5];
  char* ws = (char*)d_ws;
  (void)hipFuncSetAttribute((const void*)cn_k1, hipFuncAttributeMaxDynamicSharedMemorySize, K1_LDS);
  (void)hipFuncSetAttribute((const void*)cn_k2, hipFuncAttributeMaxDynamicSharedMemorySize, K2_LDS);
  cn_k1<<<256, 832, K1_LDS, stream>>>(x, c, w1, wc, w2, ws);
  cn_k2<<<256, 256, K2_LDS, stream>>>(ws, xf, (u16*)d_out);
}
```

```cpp
#define RLOC 0
#define GATE 0
#include <hip/hip_runtime.h>
#include <hip/hip_bf16.h>
typedef unsigned short u16;
typedef unsigned v4u __attribute__((ext_vector_type(4)));
typedef unsigned v2u __attribute__((ext_vector_type(2)));
typedef short v4s __attribute__((ext_vector_type(4)));
typedef __bf16 v8bf __attribute__((ext_vector_type(8)));
typedef __bf16 v2bf __attribute__((ext_vector_type(2)));
typedef float f32x2 __attribute__((ext_vector_type(2)));
typedef float f32x4 __attribute__((ext_vector_type(4)));
#define GAS __attribute__((address_space(1)))
#define LAS __attribute__((address_space(3)))
#define DI __device__ __forceinline__
#define AI __attribute__((always_inline))
#define SB() __builtin_amdgcn_sched_barrier(0)
#define CB() asm volatile("" ::: "memory")

constexpr int WS_EPOCH = 0;
constexpr int WS_STATS = 16384;
constexpr int WS_HN = WS_STATS + 262144;
constexpr int WS_HIN = WS_HN + 2097152;
constexpr int WS_H2 = WS_HIN + 2097152;
constexpr int WS_WCT = WS_H2 + 2097152;
constexpr int WS_W2T = WS_WCT + 4718592;
constexpr int WS_END = WS_W2T + 524288;
constexpr int RSRC_W3 = 0x00020000;
constexpr int SC1 = 16;
constexpr unsigned SPIN_CAP = 1u << 17;
constexpr unsigned POISON = 0xFFFFFFFFu;

DI float asf(unsigned u) { return __builtin_bit_cast(float, u); }
DI unsigned asu(float f) { return __builtin_bit_cast(unsigned, f); }
DI unsigned pk_bf16(float a, float b) { f32x2 v = {a, b}; return __builtin_bit_cast(unsigned, __builtin_convertvector(v, v2bf)); }
DI float bf_lo(unsigned u) { return asf(u << 16); }
DI float bf_hi(unsigned u) { return asf(u & 0xffff0000u); }
DI unsigned umin(unsigned a, unsigned b) { return a < b ? a : b; }
DI unsigned umax(unsigned a, unsigned b) { return a > b ? a : b; }
DI f32x4 mfma16(v4u a, v4u b, f32x4 c) { return __builtin_amdgcn_mfma_f32_16x16x32_bf16(__builtin_bit_cast(v8bf, a), __builtin_bit_cast(v8bf, b), c, 0, 0, 0); }
DI float xsum16(float v) { auto r = __builtin_amdgcn_permlane16_swap(asu(v), asu(v), false, false); return asf(r[0]) + asf(r[1]); }
DI float xsum32(float v) { auto r = __builtin_amdgcn_permlane32_swap(asu(v), asu(v), false, false); return asf(r[0]) + asf(r[1]); }
DI v4u widen16(v2u a, v2u b) {
  auto rx = __builtin_amdgcn_permlane16_swap(a.x, b.x, false, false);
  auto ry = __builtin_amdgcn_permlane16_swap(a.y, b.y, false, false);
  v4u o = {rx[0], ry[0], rx[1], ry[1]};
  return o;
}
DI v4u transpose4(unsigned a0, unsigned a1, unsigned a2, unsigned a3) {
  auto p = __builtin_amdgcn_permlane16_swap(a0, a1, false, false);
  auto q = __builtin_amdgcn_permlane16_swap(a2, a3, false, false);
  auto r = __builtin_amdgcn_permlane32_swap(p[0], q[0], false, false);
  auto s = __builtin_amdgcn_permlane32_swap(p[1], q[1], false, false);
  v4u o = {r[0], s[0], r[1], s[1]};
  return o;
}
DI v4u trfrag(LAS char* p0, LAS char* p1) {
  v4s a0 = __builtin_amdgcn_ds_read_tr16_b64_v4i16((LAS v4s*)p0);
  v4s a1 = __builtin_amdgcn_ds_read_tr16_b64_v4i16((LAS v4s*)p1);
  return __builtin_bit_cast(v4u, __builtin_shufflevector(a0, a1, 0, 1, 2, 3, 4, 5, 6, 7));
}
template <int V> struct IC { static constexpr int v = V; };
template <int B, int E, class F> DI void static_for(F&& f) { if constexpr (B < E) { f(IC<B>{}); static_for<B + 1, E>(f); } }
#define BARRIER() do { CB(); __builtin_amdgcn_s_barrier(); CB(); } while (0)
template <int N> DI void wait_vm() { asm volatile("s_waitcnt vmcnt(%0)" ::"i"(N) : "memory"); }
DI void wait_lgkm0() { asm volatile("s_waitcnt lgkmcnt(0)" ::: "memory"); }
DI v4u mk_rsrc(const void* p, unsigned bytes) { unsigned long long a = (unsigned long long)p; v4u r = {(unsigned)a, (unsigned)(a >> 32) & 0xffffu, bytes, (unsigned)RSRC_W3}; return r; }
DI void dma16(v4u rsrc, int voff, int soff, int lds) {
  asm volatile("s_mov_b32 m0, %0\n\ts_nop 0\n\tbuffer_load_dwordx4 %1, %2, %3 offen lds" ::"s"(lds), "v"(voff), "s"(rsrc), "s"(soff) : "memory");
}
#define ST128(val, rs, v, aux) __builtin_amdgcn_raw_buffer_store_b128(val, rs, v, 0, aux)
#define LD128(rs, v, s, aux) __builtin_amdgcn_raw_buffer_load_b128(rs, v, s, aux)
#define ST32(val, rs, v, aux) __builtin_amdgcn_raw_buffer_store_b32(val, rs, v, 0, aux)

constexpr int L_C = 0;
constexpr int L_W = 65536;
constexpr int L_X = 131072;
constexpr int K1_LDS = 139264;
#define INV 0
#define G_SLEEP 0
#define AUX_WT 16
#define AUX_PO 16
#define R_AT 4
#define R_N 8
#define R_PO 4
#define XPRE 16
extern "C" __global__ __launch_bounds__(832, 4) void cn_k1(const u16* __restrict__ x, const u16* __restrict__ c, const u16* __restrict__ w1,
                                                           const u16* __restrict__ wc, const u16* __restrict__ w2, char* ws) {
  extern __shared__ __attribute__((aligned(16))) char smem_[];
  LAS char* smem = (LAS char*)smem_;
  const int bid = blockIdx.x, lane = threadIdx.x & 63, wv = __builtin_amdgcn_readfirstlane(threadIdx.x >> 6);
  const int idx = bid >> 3, pt = (bid & 7) * 4 + (idx >> 3), ct = idx & 7;
  const __amdgpu_buffer_rsrc_t rs = __builtin_amdgcn_make_buffer_rsrc((void*)ws, 0, WS_END, RSRC_W3);
  if (__builtin_expect(wv < 8, 1)) {
    const int w = wv & 3;
    const int r8 = lane >> 3, s8 = lane & 7;
    const int voc = (pt * 64 + (w >> 1) + 2 * (8 * (w & 1) + r8)) * 1024 + ((s8 ^ (4 * (w & 1) + (r8 >> 1))) << 4);
    const int vow = (8 * w + r8) * 1024 + ct * 128 + ((s8 ^ (r8 & 2) ^ ((w & 1) << 2)) << 4);
    const int rot = 4 * ((ct ^ (idx >> 3)) & 1);
    const v4u rc = mk_rsrc(c, 2097152), rw1 = mk_rsrc(w1, 524288);
    {
      const int hi = (wv >> 2) * 32768;
#pragma unroll
      for (int i = 0; i < 4; ++i) {
        const int kc = i ^ rot;
        dma16(rc, voc, hi + kc * 128, L_C + i * 8192 + wv * 1024);
        dma16(rw1, vow, hi + kc * 65536, L_W + i * 8192 + wv * 1024);
      }
    }
    if (__builtin_expect(wv >= 4, 1)) {
      auto issue = [&](int i) {
        const int kc = i ^ rot;
        dma16(rc, voc, kc * 128, L_C + i * 8192 + w * 1024);
        dma16(rw1, vow, kc * 65536, L_W + i * 8192 + w * 1024);
        dma16(rc, voc, 32768 + kc * 128, L_C + i * 8192 + 4096 + w * 1024);
        dma16(rw1, vow, 32768 + kc * 65536, L_W + i * 8192 + 4096 + w * 1024);
      };
      const v4u rx = mk_rsrc(x, 2097152);
      dma16(rx, voc, ct * 128, L_X + w * 1024);
      dma16(rx, voc, 32768 + ct * 128, L_X + 4096 + w * 1024);
      wait_vm<8>(); BARRIER(); issue(4);
      wait_vm<10>(); BARRIER(); issue(5);
      wait_vm<12>(); BARRIER(); issue(6);
      wait_vm<14>(); BARRIER(); issue(7);
      wait_vm<12>(); BARRIER();
      wait_vm<8>(); BARRIER();
      wait_vm<4>(); BARRIER();
      wait_vm<0>(); BARRIER();
      return;
    }
    const int px = lane & 15, g = lane >> 4, q = (lane & 15) >> 2, p = lane & 3;
    const int row = 16 * w + px;
    const int soff = WS_STATS + pt * 8192 + row * 16;
    unsigned epoch_;
    asm volatile("s_load_dword %0, %1, %2" : "=&s"(epoch_) : "s"(ws), "s"(WS_EPOCH + (pt * 4 + w) * 128) : "memory");
    LAS char* cb0 = smem + L_C + row * 128 + ((g ^ ((px >> 1) & 7)) << 4);
    LAS char* cb1 = smem + L_C + row * 128 + (((4 + g) ^ ((px >> 1) & 7)) << 4);
    LAS char* wb[4];
#pragma unroll
    for (int cc = 0; cc < 4; ++cc) wb[cc] = smem + L_W + (8 * g + q) * 128 + (((2 * cc + (p >> 1)) ^ ((q & 2) | ((g & 1) << 2))) << 4) + 8 * (p & 1);
    f32x4 acc[4] = {};
    v4u bq[2][2], aq[2][2][4];
    v2u xv[4];
#pragma unroll
    for (int kc = 0; kc <= 8; ++kc) {
      if (kc == 0) { asm volatile("s_waitcnt vmcnt(6) lgkmcnt(0)" : "+s"(epoch_) :: "memory"); }
      if (kc == 1) wait_vm<4>();
      if (kc == 2) wait_vm<2>();
      if (kc == 3) wait_vm<0>();
      if (kc < 8) BARRIER();
#pragma unroll
      for (int s = 0; s < 2; ++s) {
        if (kc < 8) bq[kc & 1][s] = *(LAS v4u*)((s ? cb1 : cb0) + kc * 8192);
#pragma unroll
        for (int cc = 0; cc < 4; ++cc) {
          if (kc < 8) aq[kc & 1][s][cc] = trfrag(wb[cc] + kc * 8192 + s * 4096, wb[cc] + kc * 8192 + s * 4096 + 512);
          if (kc == 5 && s == 1) xv[cc] = *(LAS v2u*)(smem + L_X + row * 128 + (((2 * cc + (g >> 1)) ^ ((px >> 1) & 7)) << 4) + 8 * (g & 1));
          if (kc > 0) acc[cc] = mfma16(aq[(kc - 1) & 1][s][cc], bq[(kc - 1) & 1][s], acc[cc]);
          SB();
        }
      }
      if (kc == 6) {
#pragma unroll
        for (int cc = 0; cc < 4; ++cc) { acc[cc][0] += bf_lo(xv[cc].x); acc[cc][1] += bf_hi(xv[cc].x); acc[cc][2] += bf_lo(xv[cc].y); acc[cc][3] += bf_hi(xv[cc].y); }
        SB();
      }
    }
    const unsigned tag = epoch_ + 1u;
    float v[16]; v2u hin[4];
    float sa = 0.f, sb = 0.f, qa = 0.f, qb = 0.f;
#pragma unroll
    for (int cc = 0; cc < 4; ++cc) {
      hin[cc].x = pk_bf16(acc[cc][0], acc[cc][1]);
      hin[cc].y = pk_bf16(acc[cc][2], acc[cc][3]);
      v[4 * cc] = bf_lo(hin[cc].x); v[4 * cc + 1] = bf_hi(hin[cc].x); v[4 * cc + 2] = bf_lo(hin[cc].y); v[4 * cc + 3] = bf_hi(hin[cc].y);
      sa += v[4 * cc] + v[4 * cc + 2]; sb += v[4 * cc + 1] + v[4 * cc + 3];
      qa += v[4 * cc] * v[4 * cc]; qb += v[4 * cc + 1] * v[4 * cc + 1]; qa += v[4 * cc + 2] * v[4 * cc + 2]; qb += v[4 * cc + 3] * v[4 * cc + 3];
    }
    const float s64 = xsum32(xsum16(sa + sb)), q64 = xsum32(xsum16(qa + qb));
    if (g == 0) { v4u gr = {asu(s64), tag, asu(q64), tag}; ST128(gr, rs, soff + ct * 1024, SC1); }
    SB();
    if (GATE == 1) BARRIER();
    const int fbase = (2 * ct * 128 + 4 * pt + w) * 1024;
    const v4u hin_a = transpose4(hin[0].x, hin[1].x, hin[2].x, hin[3].x), hin_b = transpose4(hin[0].y, hin[1].y, hin[2].y, hin[3].y);
    v4u g0, g1;
    __builtin_amdgcn_s_sleep(XPRE);
    for (unsigned spin = 0;; ++spin) {
      CB();
      g0 = LD128(rs, soff + g * 2048, 0, SC1); g1 = LD128(rs, soff + g * 2048 + 1024, 0, SC1);
      if (__all((g0.y == tag) & (g0.w == tag) & (g1.y == tag) & (g1.w == tag)) || spin > SPIN_CAP) break;
    }
    if (GATE == 2) BARRIER();
    ST128(hin_a, rs, lane * 16 + WS_HIN + fbase, SC1);
    ST128(hin_b, rs, lane * 16 + WS_HIN + fbase + 131072, SC1);
    if (ct == 0 && lane == 0) ST32(tag, rs, WS_EPOCH + (pt * 4 + w) * 128, SC1);
    const float mean = xsum32(xsum16(asf(g0.x) + asf(g1.x))) * (1.f / 512.f);
    const float ex2 = xsum32(xsum16(asf(g0.z) + asf(g1.z))) * (1.f / 512.f);
    const float rstd = __builtin_amdgcn_rsqf(fmaxf(ex2 - mean * mean, 0.f) + 1e-5f);
    const float nb = -mean * rstd;
    const float rs2 = rstd * -1.44269504f, nb2 = nb * -1.44269504f;
    const int poff = (((g >> 1) + 2 * (g & 1)) * 16 + px) * 16;
    v2u hv[4];
#pragma unroll
    for (int cc = 0; cc < 4; ++cc) {
      float o[4];
#pragma unroll
      for (int i = 0; i < 4; ++i) { const float y = v[4 * cc + i] * rstd + nb; o[i] = y * __builtin_amdgcn_rcpf(1.f + __builtin_amdgcn_exp2f(v[4 * cc + i] * rs2 + nb2)); }
      hv[cc].x = pk_bf16(o[0], o[1]); hv[cc].y = pk_bf16(o[2], o[3]);
      if (cc & 1) ST128(widen16(hv[cc - 1], hv[cc]), rs, poff + WS_HN + fbase + (cc >> 1) * 131072, SC1);
    }
    return;
  }
  {
    const int rw = wv - 8;
    const int u = bid * 5 + rw, rg = u >> 1, m = 4 * (u & 1) + (lane >> 4);
    const int coff = (u & 1) * 512 + lane * 8;
    const bool isc = rg < 576;
    const int r2 = rg - 576, s2 = r2 >> 2;
    unsigned d0, d1;
    {
      unsigned long long pc; asm volatile("s_getpc_b64 %0\ncn_code_here:" : "=s"(pc));
      int lo, len;
      asm volatile("s_mov_b32 %0, cn_k1-cn_code_here" : "=s"(lo));
      asm volatile("s_mov_b32 %0, cn_code_end-cn_k1" : "=s"(len));
      int off = (idx * 5 + rw) * 128;
      off = off + 64 < len ? off : 0;
      asm volatile("s_load_dword %0, %2, %3\n\ts_load_dword %1, %2, %4" : "=&s"(d0), "=&s"(d1) : "s"((const char*)pc + lo), "s"(off), "s"(off + 64) : "memory");
    }
    const char* src = isc ? (const char*)wc + rg * 8192 : (const char*)w2 + (64 * (s2 >> 1) + 16 * (r2 & 3) + 2 * (s2 & 1)) * 1024;
    src += coff;
    const int st2 = isc ? 2048 : 4096;
    v2u rt[8];
#pragma unroll
    for (int i = 0; i <= 8; ++i) {
      if (i >= R_AT) {
#pragma unroll
        for (int j = 0; j < 8; ++j)
          if (j / R_N == i - R_AT || (i == 8 && j / R_N > i - R_AT)) { asm volatile("global_load_dwordx2 %0, %1, off" : "=v"(rt[j]) : "v"(src + (j >> 1) * st2 + (j & 1) * 1024) : "memory"); }
      }
      if (i == 8) {
        if (GATE) BARRIER();
        __builtin_amdgcn_s_sleep(G_SLEEP);
        asm volatile("s_waitcnt vmcnt(0)" : "+v"(rt[0]), "+v"(rt[1]), "+v"(rt[2]), "+v"(rt[3]), "+v"(rt[4]), "+v"(rt[5]), "+v"(rt[6]), "+v"(rt[7]) :: "memory");
        const int fo = isc ? WS_WCT + ((((rg & 63) >> 2) * 9 + (rg >> 6)) * 2) * 1024 : WS_W2T + s2 * 4096;
        const int hs = isc ? 294912 : 2048;
        const int vo = fo + m * (isc ? 589824 : 65536) + ((rg & 3) * 16 + (lane & 15)) * 16;
        v4u o0, o1, o2, o3;
#pragma unroll
        for (int k = 0; k < 4; ++k) {
          o0[k] = __builtin_amdgcn_perm(rt[2 * k + 1].x, rt[2 * k].x, 0x05040100u); o1[k] = __builtin_amdgcn_perm(rt[2 * k + 1].x, rt[2 * k].x, 0x07060302u);
          o2[k] = __builtin_amdgcn_perm(rt[2 * k + 1].y, rt[2 * k].y, 0x05040100u); o3[k] = __builtin_amdgcn_perm(rt[2 * k + 1].y, rt[2 * k].y, 0x07060302u);
        }
        ST128(o0, rs, vo, AUX_WT); ST128(o1, rs, vo + 1024, AUX_WT); ST128(o2, rs, vo + hs, AUX_WT); ST128(o3, rs, vo + hs + 1024, AUX_WT);
        SB();
      }
      if (i == R_PO && rw < 4) {
        const v4u po = {POISON, POISON, POISON, POISON};
        ST128(po, rs, WS_H2 + bid * 8192 + rw * 2048 + lane * 16, SC1);
        ST128(po, rs, WS_H2 + bid * 8192 + rw * 2048 + 1024 + lane * 16, SC1);
        SB();
      }
      if (i < 8) BARRIER();
    }
    asm volatile("s_waitcnt lgkmcnt(0)" : "+s"(d0), "+s"(d1) :: "memory");
  }
}

#define AUXW 0
#define AUXA 0
#define AUXH2 16
#define K2_L0 14
#define K2_RN 19
#define K2_RD 4
#define W2PRE 10
constexpr int K2_LDS = 131072;
constexpr int k2_min_lead() {
  constexpr int need[30] = {0, 0, 2, 1, 4, 5, 6, 7, 12, 14, 24, 26, 36, 38, 48, 49, 52, 53, 54, 55, 84, 86, 96, 97, 100, 101, 102, 103, 132, 134};
  int mn = 1000;
  for (int l = K2_L0; l < 120; ++l) { const int ld = (l / 30) * 144 + need[l % 30] - ((l - K2_L0) * K2_RN) / K2_RD; if (ld < mn) mn = ld; }
  return mn;
}
static_assert(k2_min_lead() >= 8, "a load would be issued behind its first use");

extern "C" __global__ __launch_bounds__(256, 1) void cn_k2(char* ws, const u16* __restrict__ xf, u16* __restrict__ out) {
  extern __shared__ __attribute__((aligned(16))) char smem_[];
  LAS char* smem = (LAS char*)smem_;
  const int bid = blockIdx.x, lane = threadIdx.x & 63, w = __builtin_amdgcn_readfirstlane(threadIdx.x >> 6);
  const int xcd = bid & 7, idx = bid >> 3, b = xcd >> 2, rt = idx >> 2, n = (xcd & 3) * 4 + (idx & 3);
  const __amdgpu_buffer_rsrc_t rs = __builtin_amdgcn_make_buffer_rsrc((void*)ws, 0, WS_END, RSRC_W3);
  const int vo = lane * 16;
  const int vo_top = rt == 0 ? 0x40000000 : vo, vo_bot = rt == 7 ? 0x40000000 : vo;
  const int wbase = WS_WCT + (n * 16 + 4 * w) * 18432;
  const int abase = WS_HN + 4 * w * 131072 + (b * 32 + 4 * rt - 1) * 2048;
  v4u W[4][18], A[4][12], S[12];
  f32x4 acc[4][2][2] = {};
  const int eoff = (n * 128 + (b * 32 + 4 * rt + w) * 2) * 1024;
  const int pt = xcd * 4 + (idx >> 3), ct = idx & 7;
  const int px = lane & 15, g = lane >> 4;
  const int ooff = (pt * 64 + 32 * (w >> 1) + 2 * px + (w & 1)) * 1024 + ct * 128 + g * 32;
  const int w2base = WS_W2T + ct * 65536 + 4 * w * 4096;
  v4u hin0, hin1, xv0, xv1, W2[4][4];
  auto ldx = [&](auto lc) AI {
    constexpr int l = decltype(lc)::v;
    if constexpr (l < 120) {
      constexpr int j = l / 30, i = l % 30;
      constexpr int wi[30] = {2, -1, -1, 3, 4, 5, 0, 1, -1, -1, -1, -1, -1, -1, 8, 9, 10, 11, 6, 7, -1, -1, 14, 15, 16, 17, 12, 13, -1, -1};
      constexpr int ai[30] = {-1, 0, 1, -1, -1, -1, -1, -1, 2, 3, 4, 5, 6, 7, -1, -1, -1, -1, -1, -1, 8, 9, -1, -1, -1, -1, -1, -1, 10, 11};
      if constexpr (wi[i] >= 0) { constexpr int f = wi[i]; W[j][f] = LD128(rs, vo + (f & 3) * 1024, wbase + j * 18432 + (f >> 2) * 4096, AUXW); }
      else { constexpr int f = ai[i]; A[j][f] = LD128(rs, (f < 2 ? vo_top : f >= 10 ? vo_bot : vo) + (f & 3) * 1024, abase + j * 131072 + (f >> 2) * 4096, AUXA); }
    }
    else if constexpr (l == 120) hin0 = LD128(rs, vo, WS_HIN + eoff, AUXA);
    else if constexpr (l == 121) hin1 = LD128(rs, vo + 1024, WS_HIN + eoff, AUXA);
    else if constexpr (l < 138) { constexpr int k = l - 122; W2[k >> 2][k & 3] = LD128(rs, vo + (k & 3) * 1024, w2base + (k >> 2) * 4096, AUXW); }
    else if constexpr (l == 138) xv0 = *(const GAS v4u*)((const char*)xf + ooff);
    else if constexpr (l == 139) xv1 = *(const GAS v4u*)((const char*)xf + ooff + 16);
  };
  auto ldsb = [&](auto lc) AI { ldx(lc); SB(); };
  static_for<0, K2_L0>(ldsb);
  constexpr int LREST = K2_L0 + (576 * K2_RD + K2_RN - 1) / K2_RN;
  LAS char* wscr = smem + w * 16384 + lane * 16;
  LAS char* rscr = smem + w * 4096 + lane * 16;
  static_for<0, 48>([&](auto uc) AI {
    constexpr int u = decltype(uc)::v, j = u / 12, dy = (u / 4) % 3, o = u % 4, R = o + dy;
    constexpr bool newrow = (dy == 0) || (o == 3);
    static_for<0, 12>([&](auto mc) AI {
      constexpr int m = decltype(mc)::v, slot = u * 12 + m;
      constexpr int dl = (slot * K2_RD + K2_RN - 1) / K2_RN;
      if constexpr ((dl * K2_RN) / K2_RD == slot) ldx(IC<K2_L0 + dl>{});
      if constexpr (newrow && m < 8) {
        constexpr int k = m & 3;
        if constexpr (m < 4) S[2 * R][k] = __builtin_amdgcn_update_dpp(0u, A[j][2 * R][k], 0x101, 0xf, 0xf, true);
        else S[2 * R + 1][k] = __builtin_amdgcn_update_dpp(0u, A[j][2 * R + 1][k], 0x111, 0xf, 0xf, true);
      }
      constexpr int c2 = m & 1, t = m >> 1;
      if constexpr (t == 0) acc[o][0][c2] = mfma16(W[j][(3 * dy + 1) * 2 + c2], A[j][2 * R], acc[o][0][c2]);
      if constexpr (t == 1) acc[o][1][c2] = mfma16(W[j][(3 * dy + 1) * 2 + c2], A[j][2 * R + 1], acc[o][1][c2]);
      if constexpr (t == 2) acc[o][0][c2] = mfma16(W[j][(3 * dy + 2) * 2 + c2], A[j][2 * R + 1], acc[o][0][c2]);
      if constexpr (t == 3) acc[o][1][c2] = mfma16(W[j][(3 * dy + 0) * 2 + c2], A[j][2 * R], acc[o][1][c2]);
      if constexpr (t == 4) acc[o][1][c2] = mfma16(W[j][(3 * dy + 2) * 2 + c2], S[2 * R], acc[o][1][c2]);
      if constexpr (t == 5) acc[o][0][c2] = mfma16(W[j][(3 * dy + 0) * 2 + c2], S[2 * R + 1], acc[o][0][c2]);
      SB();
      if constexpr (u > 44 && m >= 4 && m < 8) *(LAS f32x4*)(wscr + ((o - 1) * 4 + m - 4) * 1024) = acc[o - 1][(m - 4) >> 1][m & 1];
    });
  });
#pragma unroll
  for (int f = 0; f < 4; ++f) *(LAS f32x4*)(wscr + (12 + f) * 1024) = acc[3][f >> 1][f & 1];
#pragma unroll
  for (int f = 0; f < 12; ++f) asm volatile("" :: "a"(acc[f >> 2][(f >> 1) & 1][f & 1]));
  static_for<LREST, LREST + 3>(ldsb);
  wait_lgkm0();
  BARRIER();
  {
    f32x4 t[4][4];
#pragma unroll
    for (int f = 0; f < 4; ++f)
#pragma unroll
      for (int s = 0; s < 4; ++s) t[f][s] = *(LAS f32x4*)(rscr + s * 16384 + f * 1024);
    SB();
    static_for<LREST + 3, 140>(ldsb);
#pragma unroll
    for (int par = 0; par < 2; ++par) {
      const f32x4 t0 = (t[2 * par][0] + t[2 * par][1]) + (t[2 * par][2] + t[2 * par][3]);
      const f32x4 t1 = (t[2 * par + 1][0] + t[2 * par + 1][1]) + (t[2 * par + 1][2] + t[2 * par + 1][3]);
      const v4u hi = par ? hin1 : hin0;
      v4u o = {pk_bf16(t0[0] + bf_lo(hi.x), t1[0] + bf_hi(hi.x)), pk_bf16(t0[1] + bf_lo(hi.y), t1[1] + bf_hi(hi.y)),
               pk_bf16(t0[2] + bf_lo(hi.z), t1[2] + bf_hi(hi.z)), pk_bf16(t0[3] + bf_lo(hi.w), t1[3] + bf_hi(hi.w))};
#pragma unroll
      for (int i = 0; i < 4; ++i) o[i] = umin(o[i], POISON - 1u);
      ST128(o, rs, vo + par * 1024 + WS_H2 + eoff, SC1);
    }
  }
#pragma unroll
  for (int k = 0; k < 16; ++k) *(LAS v4u*)(smem + 65536 + w * 16384 + k * 1024 + lane * 16) = W2[k >> 2][k & 3];
  wait_lgkm0();
  BARRIER();
  {
    const int a2base = WS_H2 + pt * 4096 + w * 1024;
    f32x4 r[4];
#pragma unroll 1
    for (unsigned spin = 0;; ++spin) {
      CB();
      v4u A2[16], WF[16][4];
#pragma unroll
      for (int s = 0; s < 16; ++s) {
        A2[s] = LD128(rs, vo, a2base + s * 131072, AUXH2);
        if (s < W2PRE) {
#pragma unroll
          for (int t = 0; t < 4; ++t) WF[s][t] = *(LAS v4u*)(smem + 65536 + (s * 4 + t) * 1024 + lane * 16);
        }
        SB();
      }
      unsigned mx = 0;
#pragma unroll
      for (int s = 0; s < 16; ++s)
#pragma unroll
        for (int t = 0; t < 4; ++t) {
          const f32x4 zero = {0.f, 0.f, 0.f, 0.f};
          if (s + W2PRE < 16) WF[s + W2PRE][t] = *(LAS v4u*)(smem + 65536 + ((s + W2PRE) * 4 + t) * 1024 + lane * 16);
          r[t] = mfma16(WF[s][t], A2[s], s == 0 ? zero : r[t]);
          if (t == 0) mx = umax(umax(mx, A2[s].x), A2[s].y);
          if (t == 1) mx = umax(umax(mx, A2[s].z), A2[s].w);
          SB();
        }
      if (__builtin_expect(__all(mx != POISON), 1) || spin > SPIN_CAP) break;
    }
    v4u o0 = {pk_bf16(r[0][0] + bf_lo(xv0.x), r[1][0] + bf_hi(xv0.x)), pk_bf16(r[2][0] + bf_lo(xv0.y), r[3][0] + bf_hi(xv0.y)),
              pk_bf16(r[0][1] + bf_lo(xv0.z), r[1][1] + bf_hi(xv0.z)), pk_bf16(r[2][1] + bf_lo(xv0.w), r[3][1] + bf_hi(xv0.w))};
    v4u o1 = {pk_bf16(r[0][2] + bf_lo(xv1.x), r[1][2] + bf_hi(xv1.x)), pk_bf16(r[2][2] + bf_lo(xv1.y), r[3][2] + bf_hi(xv1.y)),
              pk_bf16(r[0][3] + bf_lo(xv1.z), r[1][3] + bf_hi(xv1.z)), pk_bf16(r[2][3] + bf_lo(xv1.w), r[3][3] + bf_hi(xv1.w))};
    *(GAS v4u*)((char*)out + ooff) = o0;
    *(GAS v4u*)((char*)out + ooff + 16) = o1;
  }
  asm volatile("cn_code_end:");
}

extern "C" void kernel_launch(void* const* d_in, const int* in_sizes, int n_in, void* d_out, int out_size, void* d_ws, size_t ws_size, hipStream_t stream) {
  const u16* xf = (const u16*)d_in[0]; const u16* x = (const u16*)d_in[1]; const u16* c = (const u16*)d_in[2];
  const u16* w1 = (const u16*)d_in[3]; const u16* w2 = (const u16*)d_in[4]; const u16* wc = (const u16*)d_in[5];
  char* ws = (char*)d_ws;
  (void)hipFuncSetAttribute((const void*)cn_k1, hipFuncAttributeMaxDynamicSharedMemorySize, K1_LDS);
  (void)hipFuncSetAttribute((const void*)cn_k2, hipFuncAttributeMaxDynamicSharedMemorySize, K2_LDS);
  cn_k1<<<256, 832, K1_LDS, stream>>>(x, c, w1, wc, w2, ws);
  cn_k2<<<256, 256, K2_LDS, stream>>>(ws, xf, (u16*)d_out);
}
```

```cpp
#include <hip/hip_runtime.h>
#include <hip/hip_bf16.h>
typedef unsigned short u16;
typedef unsigned v4u __attribute__((ext_vector_type(4)));
typedef unsigned v2u __attribute__((ext_vector_type(2)));
typedef short v4s __attribute__((ext_vector_type(4)));
typedef __bf16 v8bf __attribute__((ext_vector_type(8)));
typedef __bf16 v2bf __attribute__((ext_vector_type(2)));
typedef float f32x2 __attribute__((ext_vector_type(2)));
typedef float f32x4 __attribute__((ext_vector_type(4)));
#define GAS __attribute__((address_space(1)))
#define LAS __attribute__((address_space(3)))
#define DI __device__ __forceinline__
#define AI __attribute__((always_inline))
#define SB() __builtin_amdgcn_sched_barrier(0)
#define CB() asm volatile("" ::: "memory")

constexpr int WS_EPOCH = 0;
constexpr int WS_STATS = 16384;
constexpr int WS_HN = WS_STATS + 262144;
constexpr int WS_HIN = WS_HN + 2097152;
constexpr int WS_H2 = WS_HIN + 2097152;
constexpr int WS_WCT = WS_H2 + 2097152;
constexpr int WS_W2T = WS_WCT + 4718592;
constexpr int WS_END = WS_W2T + 524288;
constexpr int RSRC_W3 = 0x00020000;
constexpr int SC1 = 16;
constexpr unsigned SPIN_CAP = 1u << 17;
constexpr unsigned POISON = 0xFFFFFFFFu;

DI float asf(unsigned u) { return __builtin_bit_cast(float, u); }
DI unsigned asu(float f) { return __builtin_bit_cast(unsigned, f); }
DI unsigned pk_bf16(float a, float b) { f32x2 v = {a, b}; return __builtin_bit_cast(unsigned, __builtin_convertvector(v, v2bf)); }
DI float bf_lo(unsigned u) { return asf(u << 16); }
DI float bf_hi(unsigned u) { return asf(u & 0xffff0000u); }
DI unsigned umin(unsigned a, unsigned b) { return a < b ? a : b; }
DI unsigned umax(unsigned a, unsigned b) { return a > b ? a : b; }
DI f32x4 mfma16(v4u a, v4u b, f32x4 c) { return __builtin_amdgcn_mfma_f32_16x16x32_bf16(__builtin_bit_cast(v8bf, a), __builtin_bit_cast(v8bf, b), c, 0, 0, 0); }
DI float xsum16(float v) { auto r = __builtin_amdgcn_permlane16_swap(asu(v), asu(v), false, false); return asf(r[0]) + asf(r[1]); }
DI float xsum32(float v) { auto r = __builtin_amdgcn_permlane32_swap(asu(v), asu(v), false, false); return asf(r[0]) + asf(r[1]); }
DI v4u widen16(v2u a, v2u b) {
  auto rx = __builtin_amdgcn_permlane16_swap(a.x, b.x, false, false);
  auto ry = __builtin_amdgcn_permlane16_swap(a.y, b.y, false, false);
  v4u o = {rx[0], ry[0], rx[1], ry[1]};
  return o;
}
DI v4u transpose4(unsigned a0, unsigned a1, unsigned a2, unsigned a3) {
  auto p = __builtin_amdgcn_permlane16_swap(a0, a1, false, false);
  auto q = __builtin_amdgcn_permlane16_swap(a2, a3, false, false);
  auto r = __builtin_amdgcn_permlane32_swap(p[0], q[0], false, false);
  auto s = __builtin_amdgcn_permlane32_swap(p[1], q[1], false, false);
  v4u o = {r[0], s[0], r[1], s[1]};
  return o;
}
DI v4u trfrag(LAS char* p0, LAS char* p1) {
  v4s a0 = __builtin_amdgcn_ds_read_tr16_b64_v4i16((LAS v4s*)p0);
  v4s a1 = __builtin_amdgcn_ds_read_tr16_b64_v4i16((LAS v4s*)p1);
  return __builtin_bit_cast(v4u, __builtin_shufflevector(a0, a1, 0, 1, 2, 3, 4, 5, 6, 7));
}
template <int V> struct IC { static constexpr int v = V; };
template <int B, int E, class F> DI void static_for(F&& f) { if constexpr (B < E) { f(IC<B>{}); static_for<B + 1, E>(f); } }
#define BARRIER() do { CB(); __builtin_amdgcn_s_barrier(); CB(); } while (0)
template <int N> DI void wait_vm() { asm volatile("s_waitcnt vmcnt(%0)" ::"i"(N) : "memory"); }
DI void wait_lgkm0() { asm volatile("s_waitcnt lgkmcnt(0)" ::: "memory"); }
DI v4u mk_rsrc(const void* p, unsigned bytes) { unsigned long long a = (unsigned long long)p; v4u r = {(unsigned)a, (unsigned)(a >> 32) & 0xffffu, bytes, (unsigned)RSRC_W3}; return r; }
DI void dma16(v4u rsrc, int voff, int soff, int lds) {
  asm volatile("s_mov_b32 m0, %0\n\ts_nop 0\n\tbuffer_load_dwordx4 %1, %2, %3 offen lds" ::"s"(lds), "v"(voff), "s"(rsrc), "s"(soff) : "memory");
}
#define ST128(val, rs, v, aux) __builtin_amdgcn_raw_buffer_store_b128(val, rs, v, 0, aux)
#define LD128(rs, v, s, aux) __builtin_amdgcn_raw_buffer_load_b128(rs, v, s, aux)
#define ST32(val, rs, v, aux) __builtin_amdgcn_raw_buffer_store_b32(val, rs, v, 0, aux)

constexpr int L_C = 0;
constexpr int L_W = 65536;
constexpr int L_X = 131072;
constexpr int K1_LDS = 139264;
#define POLOC 1
#define NOW2 1
#define R_AT 4
#define R_PO 4
#define XPRE 16
extern "C" __global__ __launch_bounds__(832, 4) void cn_k1(const u16* __restrict__ x, const u16* __restrict__ c, const u16* __restrict__ w1,
                                                           const u16* __restrict__ wc, const u16* __restrict__ w2, char* ws) {
  extern __shared__ __attribute__((aligned(16))) char smem_[];
  LAS char* smem = (LAS char*)smem_;
  const int bid = blockIdx.x, lane = threadIdx.x & 63, wv = __builtin_amdgcn_readfirstlane(threadIdx.x >> 6);
  const int idx = bid >> 3, pt = (bid & 7) * 4 + (idx >> 3), ct = idx & 7;
  const __amdgpu_buffer_rsrc_t rs = __builtin_amdgcn_make_buffer_rsrc((void*)ws, 0, WS_END, RSRC_W3);
  if (__builtin_expect(wv < 8, 1)) {
    const int w = wv & 3;
    const int r8 = lane >> 3, s8 = lane & 7;
    const int voc = (pt * 64 + (w >> 1) + 2 * (8 * (w & 1) + r8)) * 1024 + ((s8 ^ (4 * (w & 1) + (r8 >> 1))) << 4);
    const int vow = (8 * w + r8) * 1024 + ct * 128 + ((s8 ^ (r8 & 2) ^ ((w & 1) << 2)) << 4);
    const int rot = 4 * ((ct ^ (idx >> 3)) & 1);
    const v4u rc = mk_rsrc(c, 2097152), rw1 = mk_rsrc(w1, 524288);
    {
      const int hi = (wv >> 2) * 32768;
#pragma unroll
      for (int i = 0; i < 4; ++i) {
        const int kc = i ^ rot;
        dma16(rc, voc, hi + kc * 128, L_C + i * 8192 + wv * 1024);
        dma16(rw1, vow, hi + kc * 65536, L_W + i * 8192 + wv * 1024);
      }
    }
    if (__builtin_expect(wv >= 4, 1)) {
      auto issue = [&](int i) {
        const int kc = i ^ rot;
        dma16(rc, voc, kc * 128, L_C + i * 8192 + w * 1024);
        dma16(rw1, vow, kc * 65536, L_W + i * 8192 + w * 1024);
        dma16(rc, voc, 32768 + kc * 128, L_C + i * 8192 + 4096 + w * 1024);
        dma16(rw1, vow, 32768 + kc * 65536, L_W + i * 8192 + 4096 + w * 1024);
      };
      const v4u rx = mk_rsrc(x, 2097152);
      dma16(rx, voc, ct * 128, L_X + w * 1024);
      dma16(rx, voc, 32768 + ct * 128, L_X + 4096 + w * 1024);
      wait_vm<8>(); BARRIER(); issue(4);
      wait_vm<10>(); BARRIER(); issue(5);
      wait_vm<12>(); BARRIER(); issue(6);
      wait_vm<14>(); BARRIER(); issue(7);
      wait_vm<12>(); BARRIER();
      wait_vm<8>(); BARRIER();
      wait_vm<4>(); BARRIER();
      wait_vm<0>(); BARRIER();
      return;
    }
    const int px = lane & 15, g = lane >> 4, q = (lane & 15) >> 2, p = lane & 3;
    const int row = 16 * w + px;
    const int soff = WS_STATS + pt * 8192 + row * 16;
    unsigned epoch_;
    asm volatile("s_load_dword %0, %1, %2" : "=&s"(epoch_) : "s"(ws), "s"(WS_EPOCH + (pt * 4 + w) * 128) : "memory");
    LAS char* cb0 = smem + L_C + row * 128 + ((g ^ ((px >> 1) & 7)) << 4);
    LAS char* cb1 = smem + L_C + row * 128 + (((4 + g) ^ ((px >> 1) & 7)) << 4);
    LAS char* wb[4];
#pragma unroll
    for (int cc = 0; cc < 4; ++cc) wb[cc] = smem + L_W + (8 * g + q) * 128 + (((2 * cc + (p >> 1)) ^ ((q & 2) | ((g & 1) << 2))) << 4) + 8 * (p & 1);
    f32x4 acc[4] = {};
    v4u bq[2][2], aq[2][2][4];
    v2u xv[4];
#pragma unroll
    for (int kc = 0; kc <= 8; ++kc) {
      if (kc == 0) { asm volatile("s_waitcnt vmcnt(6) lgkmcnt(0)" : "+s"(epoch_) :: "memory"); }
      if (kc == 1) wait_vm<4>();
      if (kc == 2) wait_vm<2>();
      if (kc == 3) wait_vm<0>();
      if (kc < 8) BARRIER();
#pragma unroll
      for (int s = 0; s < 2; ++s) {
        if (kc < 8) bq[kc & 1][s] = *(LAS v4u*)((s ? cb1 : cb0) + kc * 8192);
#pragma unroll
        for (int cc = 0; cc < 4; ++cc) {
          if (kc < 8) aq[kc & 1][s][cc] = trfrag(wb[cc] + kc * 8192 + s * 4096, wb[cc] + kc * 8192 + s * 4096 + 512);
          if (kc == 5 && s == 1) xv[cc] = *(LAS v2u*)(smem + L_X + row * 128 + (((2 * cc + (g >> 1)) ^ ((px >> 1) & 7)) << 4) + 8 * (g & 1));
          if (kc > 0) acc[cc] = mfma16(aq[(kc - 1) & 1][s][cc], bq[(kc - 1) & 1][s], acc[cc]);
          SB();
        }
      }
      if (kc == 6) {
#pragma unroll
        for (int cc = 0; cc < 4; ++cc) { acc[cc][0] += bf_lo(xv[cc].x); acc[cc][1] += bf_hi(xv[cc].x); acc[cc][2] += bf_lo(xv[cc].y); acc[cc][3] += bf_hi(xv[cc].y); }
        SB();
      }
    }
    const unsigned tag = epoch_ + 1u;
    float v[16]; v2u hin[4];
    float sa = 0.f, sb = 0.f, qa = 0.f, qb = 0.f;
#pragma unroll
    for (int cc = 0; cc < 4; ++cc) {
      hin[cc].x = pk_bf16(acc[cc][0], acc[cc][1]);
      hin[cc].y = pk_bf16(acc[cc][2], acc[cc][3]);
      v[4 * cc] = bf_lo(hin[cc].x); v[4 * cc + 1] = bf_hi(hin[cc].x); v[4 * cc + 2] = bf_lo(hin[cc].y); v[4 * cc + 3] = bf_hi(hin[cc].y);
      sa += v[4 * cc] + v[4 * cc + 2]; sb += v[4 * cc + 1] + v[4 * cc + 3];
      qa += v[4 * cc] * v[4 * cc]; qb += v[4 * cc + 1] * v[4 * cc + 1]; qa += v[4 * cc + 2] * v[4 * cc + 2]; qb += v[4 * cc + 3] * v[4 * cc + 3];
    }
    const float s64 = xsum32(xsum16(sa + sb)), q64 = xsum32(xsum16(qa + qb));
    if (g == 0) { v4u gr = {asu(s64), tag, asu(q64), tag}; ST128(gr, rs, soff + ct * 1024, SC1); }
    SB();
    const int fbase = (2 * ct * 128 + 4 * pt + w) * 1024;
    const v4u hin_a = transpose4(hin[0].x, hin[1].x, hin[2].x, hin[3].x), hin_b = transpose4(hin[0].y, hin[1].y, hin[2].y, hin[3].y);
    v4u g0, g1;
    __builtin_amdgcn_s_sleep(XPRE);
    for (unsigned spin = 0;; ++spin) {
      CB();
      g0 = LD128(rs, soff + g * 2048, 0, SC1); g1 = LD128(rs, soff + g * 2048 + 1024, 0, SC1);
      if (__all((g0.y == tag) & (g0.w == tag) & (g1.y == tag) & (g1.w == tag)) || spin > SPIN_CAP) break;
    }
    ST128(hin_a, rs, lane * 16 + WS_HIN + fbase, SC1);
    ST128(hin_b, rs, lane * 16 + WS_HIN + fbase + 131072, SC1);
    if (ct == 0 && lane == 0) ST32(tag, rs, WS_EPOCH + (pt * 4 + w) * 128, SC1);
    const float mean = xsum32(xsum16(asf(g0.x) + asf(g1.x))) * (1.f / 512.f);
    const float ex2 = xsum32(xsum16(asf(g0.z) + asf(g1.z))) * (1.f / 512.f);
    const float rstd = __builtin_amdgcn_rsqf(fmaxf(ex2 - mean * mean, 0.f) + 1e-5f);
    const float nb = -mean * rstd;
    const float rs2 = rstd * -1.44269504f, nb2 = nb * -1.44269504f;
    const int poff = (((g >> 1) + 2 * (g & 1)) * 16 + px) * 16;
    v2u hv[4];
#pragma unroll
    for (int cc = 0; cc < 4; ++cc) {
      float o[4];
#pragma unroll
      for (int i = 0; i < 4; ++i) { const float y = v[4 * cc + i] * rstd + nb; o[i] = y * __builtin_amdgcn_rcpf(1.f + __builtin_amdgcn_exp2f(v[4 * cc + i] * rs2 + nb2)); }
      hv[cc].x = pk_bf16(o[0], o[1]); hv[cc].y = pk_bf16(o[2], o[3]);
      if (cc & 1) ST128(widen16(hv[cc - 1], hv[cc]), rs, poff + WS_HN + fbase + (cc >> 1) * 131072, SC1);
    }
    return;
  }
  {
    const int rw = wv - 8;
    const int u = bid * 5 + rw, rg = u >> 1, m = 4 * (u & 1) + (lane >> 4);
    const int coff = (u & 1) * 512 + lane * 8;
    const bool isc = rg < 576;
    const int r2 = rg - 576, s2 = r2 >> 2;
    unsigned d0, d1;
    {
      unsigned long long pc; asm volatile("s_getpc_b64 %0\ncn_code_here:" : "=s"(pc));
      int lo, len;
      asm volatile("s_mov_b32 %0, cn_k1-cn_code_here" : "=s"(lo));
      asm volatile("s_mov_b32 %0, cn_code_end-cn_k1" : "=s"(len));
      int off = (idx * 5 + rw) * 128;
      off = off + 64 < len ? off : 0;
      asm volatile("s_load_dword %0, %2, %3\n\ts_load_dword %1, %2, %4" : "=&s"(d0), "=&s"(d1) : "s"((const char*)pc + lo), "s"(off), "s"(off + 64) : "memory");
    }
    const char* src = isc ? (const char*)wc + rg * 8192 : (const char*)w2 + (64 * (s2 >> 1) + 16 * (r2 & 3) + 2 * (s2 & 1)) * 1024;
    src += coff;
    const int st2 = isc ? 2048 : 4096;
    v2u rt[8];
#pragma unroll
    for (int i = 0; i <= 8; ++i) {
      if (i == R_AT) {
#pragma unroll
        for (int j = 0; j < 8; ++j) { asm volatile("global_load_dwordx2 %0, %1, off" : "=v"(rt[j]) : "v"(src + (j >> 1) * st2 + (j & 1) * 1024) : "memory"); }
      }
      if (i == 8) {
        if (!(NOW2 && R_PO >= R_AT && R_PO < 8) || rw == 4) wait_vm<0>();
        asm volatile("s_waitcnt vmcnt(2)" : "+v"(rt[0]), "+v"(rt[1]), "+v"(rt[2]), "+v"(rt[3]), "+v"(rt[4]), "+v"(rt[5]), "+v"(rt[6]), "+v"(rt[7]) :: "memory");
        const int fo = isc ? WS_WCT + ((((rg & 63) >> 2) * 9 + (rg >> 6)) * 2) * 1024 : WS_W2T + s2 * 4096;
        const int hs = isc ? 294912 : 2048;
        const int vo = fo + m * (isc ? 589824 : 65536) + ((rg & 3) * 16 + (lane & 15)) * 16;
        v4u o0, o1, o2, o3;
#pragma unroll
        for (int k = 0; k < 4; ++k) {
          o0[k] = __builtin_amdgcn_perm(rt[2 * k + 1].x, rt[2 * k].x, 0x05040100u); o1[k] = __builtin_amdgcn_perm(rt[2 * k + 1].x, rt[2 * k].x, 0x07060302u);
          o2[k] = __builtin_amdgcn_perm(rt[2 * k + 1].y, rt[2 * k].y, 0x05040100u); o3[k] = __builtin_amdgcn_perm(rt[2 * k + 1].y, rt[2 * k].y, 0x07060302u);
        }
        ST128(o0, rs, vo, SC1); ST128(o1, rs, vo + 1024, SC1); ST128(o2, rs, vo + hs, SC1); ST128(o3, rs, vo + hs + 1024, SC1);
        SB();
      }
      if (i == R_PO && rw < 4) {
        const v4u po = {POISON, POISON, POISON, POISON};
        const int pob = POLOC ? WS_H2 + (idx >> 1) * 131072 + (bid & 7) * 16384 + (idx & 1) * 8192 + rw * 2048 : WS_H2 + bid * 8192 + rw * 2048;
        ST128(po, rs, pob + lane * 16, SC1);
        ST128(po, rs, pob + 1024 + lane * 16, SC1);
        SB();
      }
      if (i < 8) BARRIER();
    }
    asm volatile("s_waitcnt lgkmcnt(0)" : "+s"(d0), "+s"(d1) :: "memory");
  }
}

#define K2_L0 14
#define K2_RN 19
#define K2_RD 4
#define W2PRE 10
constexpr int K2_LDS = 131072;
constexpr int k2_min_lead() {
  constexpr int need[30] = {0, 0, 2, 1, 4, 5, 6, 7, 12, 14, 24, 26, 36, 38, 48, 49, 52, 53, 54, 55, 84, 86, 96, 97, 100, 101, 102, 103, 132, 134};
  int mn = 1000;
  for (int l = K2_L0; l < 120; ++l) { const int ld = (l / 30) * 144 + need[l % 30] - ((l - K2_L0) * K2_RN) / K2_RD; if (ld < mn) mn = ld; }
  return mn;
}
static_assert(k2_min_lead() >= 8, "a load would be issued behind its first use");

extern "C" __global__ __launch_bounds__(256, 1) void cn_k2(char* ws, const u16* __restrict__ xf, u16* __restrict__ out) {
  extern __shared__ __attribute__((aligned(16))) char smem_[];
  LAS char* smem = (LAS char*)smem_;
  const int bid = blockIdx.x, lane = threadIdx.x & 63, w = __builtin_amdgcn_readfirstlane(threadIdx.x >> 6);
  const int xcd = bid & 7, idx = bid >> 3, b = xcd >> 2, rt = idx >> 2, n = (xcd & 3) * 4 + (idx & 3);
  const __amdgpu_buffer_rsrc_t rs = __builtin_amdgcn_make_buffer_rsrc((void*)ws, 0, WS_END, RSRC_W3);
  const int vo = lane * 16;
  const int vo_top = rt == 0 ? 0x40000000 : vo, vo_bot = rt == 7 ? 0x40000000 : vo;
  const int wbase = WS_WCT + (n * 16 + 4 * w) * 18432;
  const int abase = WS_HN + 4 * w * 131072 + (b * 32 + 4 * rt - 1) * 2048;
  v4u W[4][18], A[4][12], S[12];
  f32x4 acc[4][2][2] = {};
  const int eoff = (n * 128 + (b * 32 + 4 * rt + w) * 2) * 1024;
  const int pt = xcd * 4 + (idx >> 3), ct = idx & 7;
  const int px = lane & 15, g = lane >> 4;
  const int ooff = (pt * 64 + 32 * (w >> 1) + 2 * px + (w & 1)) * 1024 + ct * 128 + g * 32;
  const int w2base = WS_W2T + ct * 65536 + 4 * w * 4096;
  v4u hin0, hin1, xv0, xv1, W2[4][4];
  auto ldx = [&](auto lc) AI {
    constexpr int l = decltype(lc)::v;
    if constexpr (l < 120) {
      constexpr int j = l / 30, i = l % 30;
      constexpr int wi[30] = {2, -1, -1, 3, 4, 5, 0, 1, -1, -1, -1, -1, -1, -1, 8, 9, 10, 11, 6, 7, -1, -1, 14, 15, 16, 17, 12, 13, -1, -1};
      constexpr int ai[30] = {-1, 0, 1, -1, -1, -1, -1, -1, 2, 3, 4, 5, 6, 7, -1, -1, -1, -1, -1, -1, 8, 9, -1, -1, -1, -1, -1, -1, 10, 11};
      if constexpr (wi[i] >= 0) { constexpr int f = wi[i]; W[j][f] = LD128(rs, vo + (f & 3) * 1024, wbase + j * 18432 + (f >> 2) * 4096, 0); }
      else { constexpr int f = ai[i]; A[j][f] = LD128(rs, (f < 2 ? vo_top : f >= 10 ? vo_bot : vo) + (f & 3) * 1024, abase + j * 131072 + (f >> 2) * 4096, 0); }
    }
    else if constexpr (l == 120) hin0 = LD128(rs, vo, WS_HIN + eoff, 0);
    else if constexpr (l == 121) hin1 = LD128(rs, vo + 1024, WS_HIN + eoff, 0);
    else if constexpr (l < 138) { constexpr int k = l - 122; W2[k >> 2][k & 3] = LD128(rs, vo + (k & 3) * 1024, w2base + (k >> 2) * 4096, 0); }
    else if constexpr (l == 138) xv0 = *(const GAS v4u*)((const char*)xf + ooff);
    else if constexpr (l == 139) xv1 = *(const GAS v4u*)((const char*)xf + ooff + 16);
  };
  auto ldsb = [&](auto lc) AI { ldx(lc); SB(); };
  static_for<0, K2_L0>(ldsb);
  constexpr int LREST = K2_L0 + (576 * K2_RD + K2_RN - 1) / K2_RN;
  LAS char* wscr = smem + w * 16384 + lane * 16;
  LAS char* rscr = smem + w * 4096 + lane * 16;
  static_for<0, 48>([&](auto uc) AI {
    constexpr int u = decltype(uc)::v, j = u / 12, dy = (u / 4) % 3, o = u % 4, R = o + dy;
    constexpr bool newrow = (dy == 0) || (o == 3);
    static_for<0, 12>([&](auto mc) AI {
      constexpr int m = decltype(mc)::v, slot = u * 12 + m;
      constexpr int dl = (slot * K2_RD + K2_RN - 1) / K2_RN;
      if constexpr ((dl * K2_RN) / K2_RD == slot) ldx(IC<K2_L0 + dl>{});
      if constexpr (newrow && m < 8) {
        constexpr int k = m & 3;
        if constexpr (m < 4) S[2 * R][k] = __builtin_amdgcn_update_dpp(0u, A[j][2 * R][k], 0x101, 0xf, 0xf, true);
        else S[2 * R + 1][k] = __builtin_amdgcn_update_dpp(0u, A[j][2 * R + 1][k], 0x111, 0xf, 0xf, true);
      }
      constexpr int c2 = m & 1, t = m >> 1;
      if constexpr (t == 0) acc[o][0][c2] = mfma16(W[j][(3 * dy + 1) * 2 + c2], A[j][2 * R], acc[o][0][c2]);
      if constexpr (t == 1) acc[o][1][c2] = mfma16(W[j][(3 * dy + 1) * 2 + c2], A[j][2 * R + 1], acc[o][1][c2]);
      if constexpr (t == 2) acc[o][0][c2] = mfma16(W[j][(3 * dy + 2) * 2 + c2], A[j][2 * R + 1], acc[o][0][c2]);
      if constexpr (t == 3) acc[o][1][c2] = mfma16(W[j][(3 * dy + 0) * 2 + c2], A[j][2 * R], acc[o][1][c2]);
      if constexpr (t == 4) acc[o][1][c2] = mfma16(W[j][(3 * dy + 2) * 2 + c2], S[2 * R], acc[o][1][c2]);
      if constexpr (t == 5) acc[o][0][c2] = mfma16(W[j][(3 * dy + 0) * 2 + c2], S[2 * R + 1], acc[o][0][c2]);
      SB();
      if constexpr (u > 44 && m >= 4 && m < 8) *(LAS f32x4*)(wscr + ((o - 1) * 4 + m - 4) * 1024) = acc[o - 1][(m - 4) >> 1][m & 1];
    });
  });
#pragma unroll
  for (int f = 0; f < 4; ++f) *(LAS f32x4*)(wscr + (12 + f) * 1024) = acc[3][f >> 1][f & 1];
#pragma unroll
  for (int f = 0; f < 12; ++f) asm volatile("" :: "a"(acc[f >> 2][(f >> 1) & 1][f & 1]));
  static_for<LREST, LREST + 3>(ldsb);
  wait_lgkm0();
  BARRIER();
  {
    f32x4 t[4][4];
#pragma unroll
    for (int f = 0; f < 4; ++f)
#pragma unroll
      for (int s = 0; s < 4; ++s) t[f][s] = *(LAS f32x4*)(rscr + s * 16384 + f * 1024);
    SB();
    static_for<LREST + 3, 140>(ldsb);
#pragma unroll
    for (int par = 0; par < 2; ++par) {
      const f32x4 t0 = (t[2 * par][0] + t[2 * par][1]) + (t[2 * par][2] + t[2 * par][3]);
      const f32x4 t1 = (t[2 * par + 1][0] + t[2 * par + 1][1]) + (t[2 * par + 1][2] + t[2 * par + 1][3]);
      const v4u hi = par ? hin1 : hin0;
      v4u o = {pk_bf16(t0[0] + bf_lo(hi.x), t1[0] + bf_hi(hi.x)), pk_bf16(t0[1] + bf_lo(hi.y), t1[1] + bf_hi(hi.y)),
               pk_bf16(t0[2] + bf_lo(hi.z), t1[2] + bf_hi(hi.z)), pk_bf16(t0[3] + bf_lo(hi.w), t1[3] + bf_hi(hi.w))};
#pragma unroll
      for (int i = 0; i < 4; ++i) o[i] = umin(o[i], POISON - 1u);
      ST128(o, rs, vo + par * 1024 + WS_H2 + eoff, SC1);
    }
  }
#pragma unroll
  for (int k = 0; k < 16; ++k) *(LAS v4u*)(smem + 65536 + w * 16384 + k * 1024 + lane * 16) = W2[k >> 2][k & 3];
  wait_lgkm0();
  BARRIER();
  {
    const int a2base = WS_H2 + pt * 4096 + w * 1024;
    f32x4 r[4];
#pragma unroll 1
    for (unsigned spin = 0;; ++spin) {
      CB();
      v4u A2[16], WF[16][4];
#pragma unroll
      for (int s = 0; s < 16; ++s) {
        A2[s] = LD128(rs, vo, a2base + s * 131072, SC1);
        if (s < W2PRE) {
#pragma unroll
          for (int t = 0; t < 4; ++t) WF[s][t] = *(LAS v4u*)(smem + 65536 + (s * 4 + t) * 1024 + lane * 16);
        }
        SB();
      }
      unsigned mx = 0;
#pragma unroll
      for (int s = 0; s < 16; ++s)
#pragma unroll
        for (int t = 0; t < 4; ++t) {
          const f32x4 zero = {0.f, 0.f, 0.f, 0.f};
          if (s + W2PRE < 16) WF[s + W2PRE][t] = *(LAS v4u*)(smem + 65536 + ((s + W2PRE) * 4 + t) * 1024 + lane * 16);
          r[t] = mfma16(WF[s][t], A2[s], s == 0 ? zero : r[t]);
          if (t == 0) mx = umax(umax(mx, A2[s].x), A2[s].y);
          if (t == 1) mx = umax(umax(mx, A2[s].z), A2[s].w);
          SB();
        }
      if (__builtin_expect(__all(mx != POISON), 1) || spin > SPIN_CAP) break;
    }
    v4u o0 = {pk_bf16(r[0][0] + bf_lo(xv0.x), r[1][0] + bf_hi(xv0.x)), pk_bf16(r[2][0] + bf_lo(xv0.y), r[3][0] + bf_hi(xv0.y)),
              pk_bf16(r[0][1] + bf_lo(xv0.z), r[1][1] + bf_hi(xv0.z)), pk_bf16(r[2][1] + bf_lo(xv0.w), r[3][1] + bf_hi(xv0.w))};
    v4u o1 = {pk_bf16(r[0][2] + bf_lo(xv1.x), r[1][2] + bf_hi(xv1.x)), pk_bf16(r[2][2] + bf_lo(xv1.y), r[3][2] + bf_hi(xv1.y)),
              pk_bf16(r[0][3] + bf_lo(xv1.z), r[1][3] + bf_hi(xv1.z)), pk_bf16(r[2][3] + bf_lo(xv1.w), r[3][3] + bf_hi(xv1.w))};
    *(GAS v4u*)((char*)out + ooff) = o0;
    *(GAS v4u*)((char*)out + ooff + 16) = o1;
  }
  asm volatile("cn_code_end:");
}

extern "C" void kernel_launch(void* const* d_in, const int* in_sizes, int n_in, void* d_out, int out_size, void* d_ws, size_t ws_size, hipStream_t stream) {
  const u16* xf = (const u16*)d_in[0]; const u16* x = (const u16*)d_in[1]; const u16* c = (const u16*)d_in[2];
  const u16* w1 = (const u16*)d_in[3]; const u16* w2 = (const u16*)d_in[4]; const u16* wc = (const u16*)d_in[5];
  char* ws = (char*)d_ws;
  (void)hipFuncSetAttribute((const void*)cn_k1, hipFuncAttributeMaxDynamicSharedMemorySize, K1_LDS);
  (void)hipFuncSetAttribute((const void*)cn_k2, hipFuncAttributeMaxDynamicSharedMemorySize, K2_LDS);
  cn_k1<<<256, 832, K1_LDS, stream>>>(x, c, w1, wc, w2, ws);
  cn_k2<<<256, 256, K2_LDS, stream>>>(ws, xf, (u16*)d_out);
}
```

```cpp
#define GATE 1
#define R_AT 6
#define R_PO 8
#include <hip/hip_runtime.h>
#include <hip/hip_bf16.h>
typedef unsigned short u16;
typedef unsigned v4u __attribute__((ext_vector_type(4)));
typedef unsigned v2u __attribute__((ext_vector_type(2)));
typedef short v4s __attribute__((ext_vector_type(4)));
typedef __bf16 v8bf __attribute__((ext_vector_type(8)));
typedef __bf16 v2bf __attribute__((ext_vector_type(2)));
typedef float f32x2 __attribute__((ext_vector_type(2)));
typedef float f32x4 __attribute__((ext_vector_type(4)));
#define GAS __attribute__((address_space(1)))
#define LAS __attribute__((address_space(3)))
#define DI __device__ __forceinline__
#define AI __attribute__((always_inline))
#define SB() __builtin_amdgcn_sched_barrier(0)
#define CB() asm volatile("" ::: "memory")

constexpr int WS_EPOCH = 0;
constexpr int WS_STATS = 16384;
constexpr int WS_HN = WS_STATS + 262144;
constexpr int WS_HIN = WS_HN + 2097152;
constexpr int WS_H2 = WS_HIN + 2097152;
constexpr int WS_WCT = WS_H2 + 2097152;
constexpr int WS_W2T = WS_WCT + 4718592;
constexpr int WS_END = WS_W2T + 524288;
constexpr int RSRC_W3 = 0x00020000;
constexpr int SC1 = 16;
constexpr unsigned SPIN_CAP = 1u << 17;
constexpr unsigned POISON = 0xFFFFFFFFu;

DI float asf(unsigned u) { return __builtin_bit_cast(float, u); }
DI unsigned asu(float f) { return __builtin_bit_cast(unsigned, f); }
DI unsigned pk_bf16(float a, float b) { f32x2 v = {a, b}; return __builtin_bit_cast(unsigned, __builtin_convertvector(v, v2bf)); }
DI float bf_lo(unsigned u) { return asf(u << 16); }
DI float bf_hi(unsigned u) { return asf(u & 0xffff0000u); }
DI unsigned umin(unsigned a, unsigned b) { return a < b ? a : b; }
DI unsigned umax(unsigned a, unsigned b) { return a > b ? a : b; }
DI f32x4 mfma16(v4u a, v4u b, f32x4 c) { return __builtin_amdgcn_mfma_f32_16x16x32_bf16(__builtin_bit_cast(v8bf, a), __builtin_bit_cast(v8bf, b), c, 0, 0, 0); }
DI float xsum16(float v) { auto r = __builtin_amdgcn_permlane16_swap(asu(v), asu(v), false, false); return asf(r[0]) + asf(r[1]); }
DI float xsum32(float v) { auto r = __builtin_amdgcn_permlane32_swap(asu(v), asu(v), false, false); return asf(r[0]) + asf(r[1]); }
DI v4u widen16(v2u a, v2u b) {
  auto rx = __builtin_amdgcn_permlane16_swap(a.x, b.x, false, false);
  auto ry = __builtin_amdgcn_permlane16_swap(a.y, b.y, false, false);
  v4u o = {rx[0], ry[0], rx[1], ry[1]};
  return o;
}
DI v4u transpose4(unsigned a0, unsigned a1, unsigned a2, unsigned a3) {
  auto p = __builtin_amdgcn_permlane16_swap(a0, a1, false, false);
  auto q = __builtin_amdgcn_permlane16_swap(a2, a3, false, false);
  auto r = __builtin_amdgcn_permlane32_swap(p[0], q[0], false, false);
  auto s = __builtin_amdgcn_permlane32_swap(p[1], q[1], false, false);
  v4u o = {r[0], s[0], r[1], s[1]};
  return o;
}
DI v4u trfrag(LAS char* p0, LAS char* p1) {
  v4s a0 = __builtin_amdgcn_ds_read_tr16_b64_v4i16((LAS v4s*)p0);
  v4s a1 = __builtin_amdgcn_ds_read_tr16_b64_v4i16((LAS v4s*)p1);
  return __builtin_bit_cast(v4u, __builtin_shufflevector(a0, a1, 0, 1, 2, 3, 4, 5, 6, 7));
}
template <int V> struct IC { static constexpr int v = V; };
template <int B, int E, class F> DI void static_for(F&& f) { if constexpr (B < E) { f(IC<B>{}); static_for<B + 1, E>(f); } }
#define BARRIER() do { CB(); __builtin_amdgcn_s_barrier(); CB(); } while (0)
template <int N> DI void wait_vm() { asm volatile("s_waitcnt vmcnt(%0)" ::"i"(N) : "memory"); }
DI void wait_lgkm0() { asm volatile("s_waitcnt lgkmcnt(0)" ::: "memory"); }
DI v4u mk_rsrc(const void* p, unsigned bytes) { unsigned long long a = (unsigned long long)p; v4u r = {(unsigned)a, (unsigned)(a >> 32) & 0xffffu, bytes, (unsigned)RSRC_W3}; return r; }
DI void dma16(v4u rsrc, int voff, int soff, int lds) {
  asm volatile("s_mov_b32 m0, %0\n\ts_nop 0\n\tbuffer_load_dwordx4 %1, %2, %3 offen lds" ::"s"(lds), "v"(voff), "s"(rsrc), "s"(soff) : "memory");
}
#define ST128(val, rs, v, aux) __builtin_amdgcn_raw_buffer_store_b128(val, rs, v, 0, aux)
#define LD128(rs, v, s, aux) __builtin_amdgcn_raw_buffer_load_b128(rs, v, s, aux)
#define ST32(val, rs, v, aux) __builtin_amdgcn_raw_buffer_store_b32(val, rs, v, 0, aux)

constexpr int L_C = 0;
constexpr int L_W = 65536;
constexpr int L_X = 131072;
constexpr int K1_LDS = 139264;
#define POLOC 1
#define NOW2 1
#define RLOC 0
#define XPRE 16
extern "C" __global__ __launch_bounds__(832, 4) void cn_k1(const u16* __restrict__ x, const u16* __restrict__ c, const u16* __restrict__ w1,
                                                           const u16* __restrict__ wc, const u16* __restrict__ w2, char* ws) {
  extern __shared__ __attribute__((aligned(16))) char smem_[];
  LAS char* smem = (LAS char*)smem_;
  const int bid = blockIdx.x, lane = threadIdx.x & 63, wv = __builtin_amdgcn_readfirstlane(threadIdx.x >> 6);
  const int idx = bid >> 3, pt = (bid & 7) * 4 + (idx >> 3), ct = idx & 7;
  const __amdgpu_buffer_rsrc_t rs = __builtin_amdgcn_make_buffer_rsrc((void*)ws, 0, WS_END, RSRC_W3);
  if (__builtin_expect(wv < 8, 1)) {
    const int w = wv & 3;
    const int r8 = lane >> 3, s8 = lane & 7;
    const int voc = (pt * 64 + (w >> 1) + 2 * (8 * (w & 1) + r8)) * 1024 + ((s8 ^ (4 * (w & 1) + (r8 >> 1))) << 4);
    const int vow = (8 * w + r8) * 1024 + ct * 128 + ((s8 ^ (r8 & 2) ^ ((w & 1) << 2)) << 4);
    const int rot = 4 * ((ct ^ (idx >> 3)) & 1);
    const v4u rc = mk_rsrc(c, 2097152), rw1 = mk_rsrc(w1, 524288);
    {
      const int hi = (wv >> 2) * 32768;
#pragma unroll
      for (int i = 0; i < 4; ++i) {
        const int kc = i ^ rot;
        dma16(rc, voc, hi + kc * 128, L_C + i * 8192 + wv * 1024);
        dma16(rw1, vow, hi + kc * 65536, L_W + i * 8192 + wv * 1024);
      }
    }
    if (__builtin_expect(wv >= 4, 1)) {
      auto issue = [&](int i) {
        const int kc = i ^ rot;
        dma16(rc, voc, kc * 128, L_C + i * 8192 + w * 1024);
        dma16(rw1, vow, kc * 65536, L_W + i * 8192 + w * 1024);
        dma16(rc, voc, 32768 + kc * 128, L_C + i * 8192 + 4096 + w * 1024);
        dma16(rw1, vow, 32768 + kc * 65536, L_W + i * 8192 + 4096 + w * 1024);
      };
      const v4u rx = mk_rsrc(x, 2097152);
      dma16(rx, voc, ct * 128, L_X + w * 1024);
      dma16(rx, voc, 32768 + ct * 128, L_X + 4096 + w * 1024);
      wait_vm<8>(); BARRIER(); issue(4);
      wait_vm<10>(); BARRIER(); issue(5);
      wait_vm<12>(); BARRIER(); issue(6);
      wait_vm<14>(); BARRIER(); issue(7);
      wait_vm<12>(); BARRIER();
      wait_vm<8>(); BARRIER();
      wait_vm<4>(); BARRIER();
      wait_vm<0>(); BARRIER();
      return;
    }
    const int px = lane & 15, g = lane >> 4, q = (lane & 15) >> 2, p = lane & 3;
    const int row = 16 * w + px;
    const int soff = WS_STATS + pt * 8192 + row * 16;
    unsigned epoch_;
    asm volatile("s_load_dword %0, %1, %2" : "=&s"(epoch_) : "s"(ws), "s"(WS_EPOCH + (pt * 4 + w) * 128) : "memory");
    LAS char* cb0 = smem + L_C + row * 128 + ((g ^ ((px >> 1) & 7)) << 4);
    LAS char* cb1 = smem + L_C + row * 128 + (((4 + g) ^ ((px >> 1) & 7)) << 4);
    LAS char* wb[4];
#pragma unroll
    for (int cc = 0; cc < 4; ++cc) wb[cc] = smem + L_W + (8 * g + q) * 128 + (((2 * cc + (p >> 1)) ^ ((q & 2) | ((g & 1) << 2))) << 4) + 8 * (p & 1);
    f32x4 acc[4] = {};
    v4u bq[2][2], aq[2][2][4];
    v2u xv[4];
#pragma unroll
    for (int kc = 0; kc <= 8; ++kc) {
      if (kc == 0) { asm volatile("s_waitcnt vmcnt(6) lgkmcnt(0)" : "+s"(epoch_) :: "memory"); }
      if (kc == 1) wait_vm<4>();
      if (kc == 2) wait_vm<2>();
      if (kc == 3) wait_vm<0>();
      if (kc < 8) BARRIER();
#pragma unroll
      for (int s = 0; s < 2; ++s) {
        if (kc < 8) bq[kc & 1][s] = *(LAS v4u*)((s ? cb1 : cb0) + kc * 8192);
#pragma unroll
        for (int cc = 0; cc < 4; ++cc) {
          if (kc < 8) aq[kc & 1][s][cc] = trfrag(wb[cc] + kc * 8192 + s * 4096, wb[cc] + kc * 8192 + s * 4096 + 512);
          if (kc == 5 && s == 1) xv[cc] = *(LAS v2u*)(smem + L_X + row * 128 + (((2 * cc + (g >> 1)) ^ ((px >> 1) & 7)) << 4) + 8 * (g & 1));
          if (kc > 0) acc[cc] = mfma16(aq[(kc - 1) & 1][s][cc], bq[(kc - 1) & 1][s], acc[cc]);
          SB();
        }
      }
      if (kc == 6) {
#pragma unroll
        for (int cc = 0; cc < 4; ++cc) { acc[cc][0] += bf_lo(xv[cc].x); acc[cc][1] += bf_hi(xv[cc].x); acc[cc][2] += bf_lo(xv[cc].y); acc[cc][3] += bf_hi(xv[cc].y); }
        SB();
      }
    }
    const unsigned tag = epoch_ + 1u;
    float v[16]; v2u hin[4];
    float sa = 0.f, sb = 0.f, qa = 0.f, qb = 0.f;
#pragma unroll
    for (int cc = 0; cc < 4; ++cc) {
      hin[cc].x = pk_bf16(acc[cc][0], acc[cc][1]);
      hin[cc].y = pk_bf16(acc[cc][2], acc[cc][3]);
      v[4 * cc] = bf_lo(hin[cc].x); v[4 * cc + 1] = bf_hi(hin[cc].x); v[4 * cc + 2] = bf_lo(hin[cc].y); v[4 * cc + 3] = bf_hi(hin[cc].y);
      sa += v[4 * cc] + v[4 * cc + 2]; sb += v[4 * cc + 1] + v[4 * cc + 3];
      qa += v[4 * cc] * v[4 * cc]; qb += v[4 * cc + 1] * v[4 * cc + 1]; qa += v[4 * cc + 2] * v[4 * cc + 2]; qb += v[4 * cc + 3] * v[4 * cc + 3];
    }
    const float s64 = xsum32(xsum16(sa + sb)), q64 = xsum32(xsum16(qa + qb));
    if (g == 0) { v4u gr = {asu(s64), tag, asu(q64), tag}; ST128(gr, rs, soff + ct * 1024, SC1); }
    SB();
    const int fbase = (2 * ct * 128 + 4 * pt + w) * 1024;
    const v4u hin_a = transpose4(hin[0].x, hin[1].x, hin[2].x, hin[3].x), hin_b = transpose4(hin[0].y, hin[1].y, hin[2].y, hin[3].y);
    v4u g0, g1;
    __builtin_amdgcn_s_sleep(XPRE);
    for (unsigned spin = 0;; ++spin) {
      CB();
      g0 = LD128(rs, soff + g * 2048, 0, SC1); g1 = LD128(rs, soff + g * 2048 + 1024, 0, SC1);
      if (__all((g0.y == tag) & (g0.w == tag) & (g1.y == tag) & (g1.w == tag)) || spin > SPIN_CAP) break;
    }
    if (GATE) BARRIER();
    ST128(hin_a, rs, lane * 16 + WS_HIN + fbase, SC1);
    ST128(hin_b, rs, lane * 16 + WS_HIN + fbase + 131072, SC1);
    if (ct == 0 && lane == 0) ST32(tag, rs, WS_EPOCH + (pt * 4 + w) * 128, SC1);
    const float mean = xsum32(xsum16(asf(g0.x) + asf(g1.x))) * (1.f / 512.f);
    const float ex2 = xsum32(xsum16(asf(g0.z) + asf(g1.z))) * (1.f / 512.f);
    const float rstd = __builtin_amdgcn_rsqf(fmaxf(ex2 - mean * mean, 0.f) + 1e-5f);
    const float nb = -mean * rstd;
    const float rs2 = rstd * -1.44269504f, nb2 = nb * -1.44269504f;
    const int poff = (((g >> 1) + 2 * (g & 1)) * 16 + px) * 16;
    v2u hv[4];
#pragma unroll
    for (int cc = 0; cc < 4; ++cc) {
      float o[4];
#pragma unroll
      for (int i = 0; i < 4; ++i) { const float y = v[4 * cc + i] * rstd + nb; o[i] = y * __builtin_amdgcn_rcpf(1.f + __builtin_amdgcn_exp2f(v[4 * cc + i] * rs2 + nb2)); }
      hv[cc].x = pk_bf16(o[0], o[1]); hv[cc].y = pk_bf16(o[2], o[3]);
      if (cc & 1) ST128(widen16(hv[cc - 1], hv[cc]), rs, poff + WS_HN + fbase + (cc >> 1) * 131072, SC1);
    }
    return;
  }
  {
    const int rw = wv - 8;
    const int u = bid * 5 + rw, rg = u >> 1, m = 4 * (u & 1) + (lane >> 4);
    const int coff = (u & 1) * 512 + lane * 8;
    const bool isc = rg < 576;
    const int r2 = rg - 576, s2 = r2 >> 2;
    unsigned d0, d1;
    {
      unsigned long long pc; asm volatile("s_getpc_b64 %0\ncn_code_here:" : "=s"(pc));
      int lo, len;
      asm volatile("s_mov_b32 %0, cn_k1-cn_code_here" : "=s"(lo));
      asm volatile("s_mov_b32 %0, cn_code_end-cn_k1" : "=s"(len));
      int off = (idx * 5 + rw) * 128;
      off = off + 64 < len ? off : 0;
      asm volatile("s_load_dword %0, %2, %3\n\ts_load_dword %1, %2, %4" : "=&s"(d0), "=&s"(d1) : "s"((const char*)pc + lo), "s"(off), "s"(off + 64) : "memory");
    }
    const char* src = isc ? (const char*)wc + rg * 8192 : (const char*)w2 + (64 * (s2 >> 1) + 16 * (r2 & 3) + 2 * (s2 & 1)) * 1024;
    src += coff;
    const int st2 = isc ? 2048 : 4096;
    v2u rt[8];
#pragma unroll
    for (int i = 0; i <= 8; ++i) {
      if (i == R_AT) {
#pragma unroll
        for (int j = 0; j < 8; ++j) { asm volatile("global_load_dwordx2 %0, %1, off" : "=v"(rt[j]) : "v"(src + (j >> 1) * st2 + (j & 1) * 1024) : "memory"); }
      }
      if (i == 8) {
        if (GATE) BARRIER();
        if (!(NOW2 && R_PO >= R_AT && R_PO < 8) || rw == 4) wait_vm<0>();
        asm volatile("s_waitcnt vmcnt(2)" : "+v"(rt[0]), "+v"(rt[1]), "+v"(rt[2]), "+v"(rt[3]), "+v"(rt[4]), "+v"(rt[5]), "+v"(rt[6]), "+v"(rt[7]) :: "memory");
        const int fo = isc ? WS_WCT + ((((rg & 63) >> 2) * 9 + (rg >> 6)) * 2) * 1024 : WS_W2T + s2 * 4096;
        const int hs = isc ? 294912 : 2048;
        const int vo = fo + m * (isc ? 589824 : 65536) + ((rg & 3) * 16 + (lane & 15)) * 16;
        v4u o0, o1, o2, o3;
#pragma unroll
        for (int k = 0; k < 4; ++k) {
          o0[k] = __builtin_amdgcn_perm(rt[2 * k + 1].x, rt[2 * k].x, 0x05040100u); o1[k] = __builtin_amdgcn_perm(rt[2 * k + 1].x, rt[2 * k].x, 0x07060302u);
          o2[k] = __builtin_amdgcn_perm(rt[2 * k + 1].y, rt[2 * k].y, 0x05040100u); o3[k] = __builtin_amdgcn_perm(rt[2 * k + 1].y, rt[2 * k].y, 0x07060302u);
        }
        ST128(o0, rs, vo, SC1); ST128(o1, rs, vo + 1024, SC1); ST128(o2, rs, vo + hs, SC1); ST128(o3, rs, vo + hs + 1024, SC1);
        SB();
      }
      if (i == R_PO && rw < 4) {
        const v4u po = {POISON, POISON, POISON, POISON};
        const int pob = POLOC ? WS_H2 + (idx >> 1) * 131072 + (bid & 7) * 16384 + (idx & 1) * 8192 + rw * 2048 : WS_H2 + bid * 8192 + rw * 2048;
        ST128(po, rs, pob + lane * 16, SC1);
        ST128(po, rs, pob + 1024 + lane * 16, SC1);
        SB();
      }
      if (i < 8) BARRIER();
    }
    asm volatile("s_waitcnt lgkmcnt(0)" : "+s"(d0), "+s"(d1) :: "memory");
  }
}

#define K2_L0 14
#define K2_RN 19
#define K2_RD 4
#define W2PRE 10
constexpr int K2_LDS = 131072;
constexpr int k2_min_lead() {
  constexpr int need[30] = {0, 0, 2, 1, 4, 5, 6, 7, 12, 14, 24, 26, 36, 38, 48, 49, 52, 53, 54, 55, 84, 86, 96, 97, 100, 101, 102, 103, 132, 134};
  int mn = 1000;
  for (int l = K2_L0; l < 120; ++l) { const int ld = (l / 30) * 144 + need[l % 30] - ((l - K2_L0) * K2_RN) / K2_RD; if (ld < mn) mn = ld; }
  return mn;
}
static_assert(k2_min_lead() >= 8, "a load would be issued behind its first use");

extern "C" __global__ __launch_bounds__(256, 1) void cn_k2(char* ws, const u16* __restrict__ xf, u16* __restrict__ out) {
  extern __shared__ __attribute__((aligned(16))) char smem_[];
  LAS char* smem = (LAS char*)smem_;
  const int bid = blockIdx.x, lane = threadIdx.x & 63, w = __builtin_amdgcn_readfirstlane(threadIdx.x >> 6);
  const int xcd = bid & 7, idx = bid >> 3, b = xcd >> 2, rt = idx >> 2, n = (xcd & 3) * 4 + (idx & 3);
  const __amdgpu_buffer_rsrc_t rs = __builtin_amdgcn_make_buffer_rsrc((void*)ws, 0, WS_END, RSRC_W3);
  const int vo = lane * 16;
  const int vo_top = rt == 0 ? 0x40000000 : vo, vo_bot = rt == 7 ? 0x40000000 : vo;
  const int wbase = WS_WCT + (n * 16 + 4 * w) * 18432;
  const int abase = WS_HN + 4 * w * 131072 + (b * 32 + 4 * rt - 1) * 2048;
  v4u W[4][18], A[4][12], S[12];
  f32x4 acc[4][2][2] = {};
  const int eoff = (n * 128 + (b * 32 + 4 * rt + w) * 2) * 1024;
  const int pt = xcd * 4 + (idx >> 3), ct = idx & 7;
  const int px = lane & 15, g = lane >> 4;
  const int ooff = (pt * 64 + 32 * (w >> 1) + 2 * px + (w & 1)) * 1024 + ct * 128 + g * 32;
  const int w2base = WS_W2T + ct * 65536 + 4 * w * 4096;
  v4u hin0, hin1, xv0, xv1, W2[4][4];
  auto ldx = [&](auto lc) AI {
    constexpr int l = decltype(lc)::v;
    if constexpr (l < 120) {
      constexpr int j = l / 30, i = l % 30;
      constexpr int wi[30] = {2, -1, -1, 3, 4, 5, 0, 1, -1, -1, -1, -1, -1, -1, 8, 9, 10, 11, 6, 7, -1, -1, 14, 15, 16, 17, 12, 13, -1, -1};
      constexpr int ai[30] = {-1, 0, 1, -1, -1, -1, -1, -1, 2, 3, 4, 5, 6, 7, -1, -1, -1, -1, -1, -1, 8, 9, -1, -1, -1, -1, -1, -1, 10, 11};
      if constexpr (wi[i] >= 0) { constexpr int f = wi[i]; W[j][f] = LD128(rs, vo + (f & 3) * 1024, wbase + j * 18432 + (f >> 2) * 4096, 0); }
      else { constexpr int f = ai[i]; A[j][f] = LD128(rs, (f < 2 ? vo_top : f >= 10 ? vo_bot : vo) + (f & 3) * 1024, abase + j * 131072 + (f >> 2) * 4096, 0); }
    }
    else if constexpr (l == 120) hin0 = LD128(rs, vo, WS_HIN + eoff, 0);
    else if constexpr (l == 121) hin1 = LD128(rs, vo + 1024, WS_HIN + eoff, 0);
    else if constexpr (l < 138) { constexpr int k = l - 122; W2[k >> 2][k & 3] = LD128(rs, vo + (k & 3) * 1024, w2base + (k >> 2) * 4096, 0); }
    else if constexpr (l == 138) xv0 = *(const GAS v4u*)((const char*)xf + ooff);
    else if constexpr (l == 139) xv1 = *(const GAS v4u*)((const char*)xf + ooff + 16);
  };
  auto ldsb = [&](auto lc) AI { ldx(lc); SB(); };
  static_for<0, K2_L0>(ldsb);
  constexpr int LREST = K2_L0 + (576 * K2_RD + K2_RN - 1) / K2_RN;
  LAS char* wscr = smem + w * 16384 + lane * 16;
  LAS char* rscr = smem + w * 4096 + lane * 16;
  static_for<0, 48>([&](auto uc) AI {
    constexpr int u = decltype(uc)::v, j = u / 12, dy = (u / 4) % 3, o = u % 4, R = o + dy;
    constexpr bool newrow = (dy == 0) || (o == 3);
    static_for<0, 12>([&](auto mc) AI {
      constexpr int m = decltype(mc)::v, slot = u * 12 + m;
      constexpr int dl = (slot * K2_RD + K2_RN - 1) / K2_RN;
      if constexpr ((dl * K2_RN) / K2_RD == slot) ldx(IC<K2_L0 + dl>{});
      if constexpr (newrow && m < 8) {
        constexpr int k = m & 3;
        if constexpr (m < 4) S[2 * R][k] = __builtin_amdgcn_update_dpp(0u, A[j][2 * R][k], 0x101, 0xf, 0xf, true);
        else S[2 * R + 1][k] = __builtin_amdgcn_update_dpp(0u, A[j][2 * R + 1][k], 0x111, 0xf, 0xf, true);
      }
      constexpr int c2 = m & 1, t = m >> 1;
      if constexpr (t == 0) acc[o][0][c2] = mfma16(W[j][(3 * dy + 1) * 2 + c2], A[j][2 * R], acc[o][0][c2]);
      if constexpr (t == 1) acc[o][1][c2] = mfma16(W[j][(3 * dy + 1) * 2 + c2], A[j][2 * R + 1], acc[o][1][c2]);
      if constexpr (t == 2) acc[o][0][c2] = mfma16(W[j][(3 * dy + 2) * 2 + c2], A[j][2 * R + 1], acc[o][0][c2]);
      if constexpr (t == 3) acc[o][1][c2] = mfma16(W[j][(3 * dy + 0) * 2 + c2], A[j][2 * R], acc[o][1][c2]);
      if constexpr (t == 4) acc[o][1][c2] = mfma16(W[j][(3 * dy + 2) * 2 + c2], S[2 * R], acc[o][1][c2]);
      if constexpr (t == 5) acc[o][0][c2] = mfma16(W[j][(3 * dy + 0) * 2 + c2], S[2 * R + 1], acc[o][0][c2]);
      SB();
      if constexpr (u > 44 && m >= 4 && m < 8) *(LAS f32x4*)(wscr + ((o - 1) * 4 + m - 4) * 1024) = acc[o - 1][(m - 4) >> 1][m & 1];
    });
  });
#pragma unroll
  for (int f = 0; f < 4; ++f) *(LAS f32x4*)(wscr + (12 + f) * 1024) = acc[3][f >> 1][f & 1];
#pragma unroll
  for (int f = 0; f < 12; ++f) asm volatile("" :: "a"(acc[f >> 2][(f >> 1) & 1][f & 1]));
  static_for<LREST, LREST + 3>(ldsb);
  wait_lgkm0();
  BARRIER();
  {
    f32x4 t[4][4];
#pragma unroll
    for (int f = 0; f < 4; ++f)
#pragma unroll
      for (int s = 0; s < 4; ++s) t[f][s] = *(LAS f32x4*)(rscr + s * 16384 + f * 1024);
    SB();
    static_for<LREST + 3, 140>(ldsb);
#pragma unroll
    for (int par = 0; par < 2; ++par) {
      const f32x4 t0 = (t[2 * par][0] + t[2 * par][1]) + (t[2 * par][2] + t[2 * par][3]);
      const f32x4 t1 = (t[2 * par + 1][0] + t[2 * par + 1][1]) + (t[2 * par + 1][2] + t[2 * par + 1][3]);
      const v4u hi = par ? hin1 : hin0;
      v4u o = {pk_bf16(t0[0] + bf_lo(hi.x), t1[0] + bf_hi(hi.x)), pk_bf16(t0[1] + bf_lo(hi.y), t1[1] + bf_hi(hi.y)),
               pk_bf16(t0[2] + bf_lo(hi.z), t1[2] + bf_hi(hi.z)), pk_bf16(t0[3] + bf_lo(hi.w), t1[3] + bf_hi(hi.w))};
#pragma unroll
      for (int i = 0; i < 4; ++i) o[i] = umin(o[i], POISON - 1u);
      ST128(o, rs, vo + par * 1024 + WS_H2 + eoff, SC1);
    }
  }
#pragma unroll
  for (int k = 0; k < 16; ++k) *(LAS v4u*)(smem + 65536 + w * 16384 + k * 1024 + lane * 16) = W2[k >> 2][k & 3];
  wait_lgkm0();
  BARRIER();
  {
    const int a2base = WS_H2 + pt * 4096 + w * 1024;
    f32x4 r[4];
#pragma unroll 1
    for (unsigned spin = 0;; ++spin) {
      CB();
      v4u A2[16], WF[16][4];
#pragma unroll
      for (int s = 0; s < 16; ++s) {
        A2[s] = LD128(rs, vo, a2base + s * 131072, SC1);
        if (s < W2PRE) {
#pragma unroll
          for (int t = 0; t < 4; ++t) WF[s][t] = *(LAS v4u*)(smem + 65536 + (s * 4 + t) * 1024 + lane * 16);
        }
        SB();
      }
      unsigned mx = 0;
#pragma unroll
      for (int s = 0; s < 16; ++s)
#pragma unroll
        for (int t = 0; t < 4; ++t) {
          const f32x4 zero = {0.f, 0.f, 0.f, 0.f};
          if (s + W2PRE < 16) WF[s + W2PRE][t] = *(LAS v4u*)(smem + 65536 + ((s + W2PRE) * 4 + t) * 1024 + lane * 16);
          r[t] = mfma16(WF[s][t], A2[s], s == 0 ? zero : r[t]);
          if (t == 0) mx = umax(umax(mx, A2[s].x), A2[s].y);
          if (t == 1) mx = umax(umax(mx, A2[s].z), A2[s].w);
          SB();
        }
      if (__builtin_expect(__all(mx != POISON), 1) || spin > SPIN_CAP) break;
    }
    v4u o0 = {pk_bf16(r[0][0] + bf_lo(xv0.x), r[1][0] + bf_hi(xv0.x)), pk_bf16(r[2][0] + bf_lo(xv0.y), r[3][0] + bf_hi(xv0.y)),
              pk_bf16(r[0][1] + bf_lo(xv0.z), r[1][1] + bf_hi(xv0.z)), pk_bf16(r[2][1] + bf_lo(xv0.w), r[3][1] + bf_hi(xv0.w))};
    v4u o1 = {pk_bf16(r[0][2] + bf_lo(xv1.x), r[1][2] + bf_hi(xv1.x)), pk_bf16(r[2][2] + bf_lo(xv1.y), r[3][2] + bf_hi(xv1.y)),
              pk_bf16(r[0][3] + bf_lo(xv1.z), r[1][3] + bf_hi(xv1.z)), pk_bf16(r[2][3] + bf_lo(xv1.w), r[3][3] + bf_hi(xv1.w))};
    *(GAS v4u*)((char*)out + ooff) = o0;
    *(GAS v4u*)((char*)out + ooff + 16) = o1;
  }
  asm volatile("cn_code_end:");
}

extern "C" void kernel_launch(void* const* d_in, const int* in_sizes, int n_in, void* d_out, int out_size, void* d_ws, size_t ws_size, hipStream_t stream) {
  const u16* xf = (const u16*)d_in[0]; const u16* x = (const u16*)d_in[1]; const u16* c = (const u16*)d_in[2];
  const u16* w1 = (const u16*)d_in[3]; const u16* w2 = (const u16*)d_in[4]; const u16* wc = (const u16*)d_in[5];
  char* ws = (char*)d_ws;
  (void)hipFuncSetAttribute((const void*)cn_k1, hipFuncAttributeMaxDynamicSharedMemorySize, K1_LDS);
  (void)hipFuncSetAttribute((const void*)cn_k2, hipFuncAttributeMaxDynamicSharedMemorySize, K2_LDS);
  cn_k1<<<256, 832, K1_LDS, stream>>>(x, c, w1, wc, w2, ws);
  cn_k2<<<256, 256, K2_LDS, stream>>>(ws, xf, (u16*)d_out);
}
```

```cpp
#define R_AT 4
#include <hip/hip_runtime.h>
#include <hip/hip_bf16.h>
typedef unsigned short u16;
typedef unsigned v4u __attribute__((ext_vector_type(4)));
typedef unsigned v2u __attribute__((ext_vector_type(2)));
typedef short v4s __attribute__((ext_vector_type(4)));
typedef __bf16 v8bf __attribute__((ext_vector_type(8)));
typedef __bf16 v2bf __attribute__((ext_vector_type(2)));
typedef float f32x2 __attribute__((ext_vector_type(2)));
typedef float f32x4 __attribute__((ext_vector_type(4)));
#define GAS __attribute__((address_space(1)))
#define LAS __attribute__((address_space(3)))
#define DI __device__ __forceinline__
#define AI __attribute__((always_inline))
#define SB() __builtin_amdgcn_sched_barrier(0)
#define CB() asm volatile("" ::: "memory")

constexpr int WS_EPOCH = 0;
constexpr int WS_STATS = 16384;
constexpr int WS_HN = WS_STATS + 262144;
constexpr int WS_HIN = WS_HN + 2097152;
constexpr int WS_H2 = WS_HIN + 2097152;
constexpr int WS_WCT = WS_H2 + 2097152;
constexpr int WS_W2T = WS_WCT + 4718592;
constexpr int WS_END = WS_W2T + 524288;
constexpr int RSRC_W3 = 0x00020000;
constexpr int SC1 = 16;
constexpr unsigned SPIN_CAP = 1u << 17;
constexpr unsigned POISON = 0xFFFFFFFFu;

DI float asf(unsigned u) { return __builtin_bit_cast(float, u); }
DI unsigned asu(float f) { return __builtin_bit_cast(unsigned, f); }
DI unsigned pk_bf16(float a, float b) { f32x2 v = {a, b}; return __builtin_bit_cast(unsigned, __builtin_convertvector(v, v2bf)); }
DI float bf_lo(unsigned u) { return asf(u << 16); }
DI float bf_hi(unsigned u) { return asf(u & 0xffff0000u); }
DI unsigned umin(unsigned a, unsigned b) { return a < b ? a : b; }
DI unsigned umax(unsigned a, unsigned b) { return a > b ? a : b; }
DI f32x4 mfma16(v4u a, v4u b, f32x4 c) { return __builtin_amdgcn_mfma_f32_16x16x32_bf16(__builtin_bit_cast(v8bf, a), __builtin_bit_cast(v8bf, b), c, 0, 0, 0); }
DI float xsum16(float v) { auto r = __builtin_amdgcn_permlane16_swap(asu(v), asu(v), false, false); return asf(r[0]) + asf(r[1]); }
DI float xsum32(float v) { auto r = __builtin_amdgcn_permlane32_swap(asu(v), asu(v), false, false); return asf(r[0]) + asf(r[1]); }
DI v4u widen16(v2u a, v2u b) {
  auto rx = __builtin_amdgcn_permlane16_swap(a.x, b.x, false, false);
  auto ry = __builtin_amdgcn_permlane16_swap(a.y, b.y, false, false);
  v4u o = {rx[0], ry[0], rx[1], ry[1]};
  return o;
}
DI v4u transpose4(unsigned a0, unsigned a1, unsigned a2, unsigned a3) {
  auto p = __builtin_amdgcn_permlane16_swap(a0, a1, false, false);
  auto q = __builtin_amdgcn_permlane16_swap(a2, a3, false, false);
  auto r = __builtin_amdgcn_permlane32_swap(p[0], q[0], false, false);
  auto s = __builtin_amdgcn_permlane32_swap(p[1], q[1], false, false);
  v4u o = {r[0], s[0], r[1], s[1]};
  return o;
}
DI v4u trfrag(LAS char* p0, LAS char* p1) {
  v4s a0 = __builtin_amdgcn_ds_read_tr16_b64_v4i16((LAS v4s*)p0);
  v4s a1 = __builtin_amdgcn_ds_read_tr16_b64_v4i16((LAS v4s*)p1);
  return __builtin_bit_cast(v4u, __builtin_shufflevector(a0, a1, 0, 1, 2, 3, 4, 5, 6, 7));
}
template <int V> struct IC { static constexpr int v = V; };
template <int B, int E, class F> DI void static_for(F&& f) { if constexpr (B < E) { f(IC<B>{}); static_for<B + 1, E>(f); } }
#define BARRIER() do { CB(); __builtin_amdgcn_s_barrier(); CB(); } while (0)
template <int N> DI void wait_vm() { asm volatile("s_waitcnt vmcnt(%0)" ::"i"(N) : "memory"); }
DI void wait_lgkm0() { asm volatile("s_waitcnt lgkmcnt(0)" ::: "memory"); }
DI v4u mk_rsrc(const void* p, unsigned bytes) { unsigned long long a = (unsigned long long)p; v4u r = {(unsigned)a, (unsigned)(a >> 32) & 0xffffu, bytes, (unsigned)RSRC_W3}; return r; }
DI void dma16(v4u rsrc, int voff, int soff, int lds) {
  asm volatile("s_mov_b32 m0, %0\n\ts_nop 0\n\tbuffer_load_dwordx4 %1, %2, %3 offen lds" ::"s"(lds), "v"(voff), "s"(rsrc), "s"(soff) : "memory");
}
#define ST128(val, rs, v, aux) __builtin_amdgcn_raw_buffer_store_b128(val, rs, v, 0, aux)
#define LD128(rs, v, s, aux) __builtin_amdgcn_raw_buffer_load_b128(rs, v, s, aux)
#define ST32(val, rs, v, aux) __builtin_amdgcn_raw_buffer_store_b32(val, rs, v, 0, aux)

constexpr int L_C = 0;
constexpr int L_W = 65536;
constexpr int L_X = 131072;
constexpr int K1_LDS = 139264;
#define XPRE 16
extern "C" __global__ __launch_bounds__(832, 4) void cn_k1(const u16* __restrict__ x, const u16* __restrict__ c, const u16* __restrict__ w1,
                                                           const u16* __restrict__ wc, const u16* __restrict__ w2, char* ws) {
  extern __shared__ __attribute__((aligned(16))) char smem_[];
  LAS char* smem = (LAS char*)smem_;
  const int bid = blockIdx.x, lane = threadIdx.x & 63, wv = __builtin_amdgcn_readfirstlane(threadIdx.x >> 6);
  const int idx = bid >> 3, pt = (bid & 7) * 4 + (idx >> 3), ct = idx & 7;
  const __amdgpu_buffer_rsrc_t rs = __builtin_amdgcn_make_buffer_rsrc((void*)ws, 0, WS_END, RSRC_W3);
  if (__builtin_expect(wv < 8, 1)) {
    const int w = wv & 3;
    const int r8 = lane >> 3, s8 = lane & 7;
    const int voc = (pt * 64 + (w >> 1) + 2 * (8 * (w & 1) + r8)) * 1024 + ((s8 ^ (4 * (w & 1) + (r8 >> 1))) << 4);
    const int vow = (8 * w + r8) * 1024 + ct * 128 + ((s8 ^ (r8 & 2) ^ ((w & 1) << 2)) << 4);
    const int rot = 4 * ((ct ^ (idx >> 3)) & 1);
    const v4u rc = mk_rsrc(c, 2097152), rw1 = mk_rsrc(w1, 524288);
    {
      const int hi = (wv >> 2) * 32768;
#pragma unroll
      for (int i = 0; i < 4; ++i) {
        const int kc = i ^ rot;
        dma16(rc, voc, hi + kc * 128, L_C + i * 8192 + wv * 1024);
        dma16(rw1, vow, hi + kc * 65536, L_W + i * 8192 + wv * 1024);
      }
    }
    if (__builtin_expect(wv >= 4, 1)) {
      auto issue = [&](int i) {
        const int kc = i ^ rot;
        dma16(rc, voc, kc * 128, L_C + i * 8192 + w * 1024);
        dma16(rw1, vow, kc * 65536, L_W + i * 8192 + w * 1024);
        dma16(rc, voc, 32768 + kc * 128, L_C + i * 8192 + 4096 + w * 1024);
        dma16(rw1, vow, 32768 + kc * 65536, L_W + i * 8192 + 4096 + w * 1024);
      };
      const v4u rx = mk_rsrc(x, 2097152);
      dma16(rx, voc, ct * 128, L_X + w * 1024);
      dma16(rx, voc, 32768 + ct * 128, L_X + 4096 + w * 1024);
      wait_vm<8>(); BARRIER(); issue(4);
      wait_vm<10>(); BARRIER(); issue(5);
      wait_vm<12>(); BARRIER(); issue(6);
      wait_vm<14>(); BARRIER(); issue(7);
      wait_vm<12>(); BARRIER();
      wait_vm<8>(); BARRIER();
      wait_vm<4>(); BARRIER();
      wait_vm<0>(); BARRIER();
      return;
    }
    const int px = lane & 15, g = lane >> 4, q = (lane & 15) >> 2, p = lane & 3;
    const int row = 16 * w + px;
    const int soff = WS_STATS + pt * 8192 + row * 16;
    unsigned epoch_;
    asm volatile("s_load_dword %0, %1, %2" : "=&s"(epoch_) : "s"(ws), "s"(WS_EPOCH + (pt * 4 + w) * 128) : "memory");
    LAS char* cb0 = smem + L_C + row * 128 + ((g ^ ((px >> 1) & 7)) << 4);
    LAS char* cb1 = smem + L_C + row * 128 + (((4 + g) ^ ((px >> 1) & 7)) << 4);
    LAS char* wb[4];
#pragma unroll
    for (int cc = 0; cc < 4; ++cc) wb[cc] = smem + L_W + (8 * g + q) * 128 + (((2 * cc + (p >> 1)) ^ ((q & 2) | ((g & 1) << 2))) << 4) + 8 * (p & 1);
    f32x4 acc[4] = {};
    v4u bq[2][2], aq[2][2][4];
    v2u xv[4];
#pragma unroll
    for (int kc = 0; kc <= 8; ++kc) {
      if (kc == 0) { asm volatile("s_waitcnt vmcnt(6) lgkmcnt(0)" : "+s"(epoch_) :: "memory"); }
      if (kc == 1) wait_vm<4>();
      if (kc == 2) wait_vm<2>();
      if (kc == 3) wait_vm<0>();
      if (kc < 8) BARRIER();
#pragma unroll
      for (int s = 0; s < 2; ++s) {
        if (kc < 8) bq[kc & 1][s] = *(LAS v4u*)((s ? cb1 : cb0) + kc * 8192);
#pragma unroll
        for (int cc = 0; cc < 4; ++cc) {
          if (kc < 8) aq[kc & 1][s][cc] = trfrag(wb[cc] + kc * 8192 + s * 4096, wb[cc] + kc * 8192 + s * 4096 + 512);
          if (kc == 5 && s == 1) xv[cc] = *(LAS v2u*)(smem + L_X + row * 128 + (((2 * cc + (g >> 1)) ^ ((px >> 1) & 7)) << 4) + 8 * (g & 1));
          if (kc > 0) acc[cc] = mfma16(aq[(kc - 1) & 1][s][cc], bq[(kc - 1) & 1][s], acc[cc]);
          SB();
        }
      }
      if (kc == 6) {
#pragma unroll
        for (int cc = 0; cc < 4; ++cc) { acc[cc][0] += bf_lo(xv[cc].x); acc[cc][1] += bf_hi(xv[cc].x); acc[cc][2] += bf_lo(xv[cc].y); acc[cc][3] += bf_hi(xv[cc].y); }
        SB();
      }
    }
    const unsigned tag = epoch_ + 1u;
    float v[16]; v2u hin[4];
    float sa = 0.f, sb = 0.f, qa = 0.f, qb = 0.f;
#pragma unroll
    for (int cc = 0; cc < 4; ++cc) {
      hin[cc].x = pk_bf16(acc[cc][0], acc[cc][1]);
      hin[cc].y = pk_bf16(acc[cc][2], acc[cc][3]);
      v[4 * cc] = bf_lo(hin[cc].x); v[4 * cc + 1] = bf_hi(hin[cc].x); v[4 * cc + 2] = bf_lo(hin[cc].y); v[4 * cc + 3] = bf_hi(hin[cc].y);
      sa += v[4 * cc] + v[4 * cc + 2]; sb += v[4 * cc + 1] + v[4 * cc + 3];
      qa += v[4 * cc] * v[4 * cc]; qb += v[4 * cc + 1] * v[4 * cc + 1]; qa += v[4 * cc + 2] * v[4 * cc + 2]; qb += v[4 * cc + 3] * v[4 * cc + 3];
    }
    const float s64 = xsum32(xsum16(sa + sb)), q64 = xsum32(xsum16(qa + qb));
    if (g == 0) { v4u gr = {asu(s64), tag, asu(q64), tag}; ST128(gr, rs, soff + ct * 1024, SC1); }
    SB();
    const int fbase = (2 * ct * 128 + 4 * pt + w) * 1024;
    const v4u hin_a = transpose4(hin[0].x, hin[1].x, hin[2].x, hin[3].x), hin_b = transpose4(hin[0].y, hin[1].y, hin[2].y, hin[3].y);
    v4u g0, g1;
    __builtin_amdgcn_s_sleep(XPRE);
    for (unsigned spin = 0;; ++spin) {
      CB();
      g0 = LD128(rs, soff + g * 2048, 0, SC1); g1 = LD128(rs, soff + g * 2048 + 1024, 0, SC1);
      if (__all((g0.y == tag) & (g0.w == tag) & (g1.y == tag) & (g1.w == tag)) || spin > SPIN_CAP) break;
    }
    BARRIER();
    ST128(hin_a, rs, lane * 16 + WS_HIN + fbase, SC1);
    ST128(hin_b, rs, lane * 16 + WS_HIN + fbase + 131072, SC1);
    if (ct == 0 && lane == 0) ST32(tag, rs, WS_EPOCH + (pt * 4 + w) * 128, SC1);
    const float mean = xsum32(xsum16(asf(g0.x) + asf(g1.x))) * (1.f / 512.f);
    const float ex2 = xsum32(xsum16(asf(g0.z) + asf(g1.z))) * (1.f / 512.f);
    const float rstd = __builtin_amdgcn_rsqf(fmaxf(ex2 - mean * mean, 0.f) + 1e-5f);
    const float nb = -mean * rstd;
    const float rs2 = rstd * -1.44269504f, nb2 = nb * -1.44269504f;
    const int poff = (((g >> 1) + 2 * (g & 1)) * 16 + px) * 16;
    v2u hv[4];
#pragma unroll
    for (int cc = 0; cc < 4; ++cc) {
      float o[4];
#pragma unroll
      for (int i = 0; i < 4; ++i) { const float y = v[4 * cc + i] * rstd + nb; o[i] = y * __builtin_amdgcn_rcpf(1.f + __builtin_amdgcn_exp2f(v[4 * cc + i] * rs2 + nb2)); }
      hv[cc].x = pk_bf16(o[0], o[1]); hv[cc].y = pk_bf16(o[2], o[3]);
      if (cc & 1) ST128(widen16(hv[cc - 1], hv[cc]), rs, poff + WS_HN + fbase + (cc >> 1) * 131072, SC1);
    }
    return;
  }
  {
    const int rw = wv - 8;
    const int u = bid * 5 + rw, rg = u >> 1, m = 4 * (u & 1) + (lane >> 4);
    const int coff = (u & 1) * 512 + lane * 8;
    const bool isc = rg < 576;
    const int r2 = rg - 576, s2 = r2 >> 2;
    unsigned d0, d1;
    {
      unsigned long long pc; asm volatile("s_getpc_b64 %0\ncn_code_here:" : "=s"(pc));
      int lo, len;
      asm volatile("s_mov_b32 %0, cn_k1-cn_code_here" : "=s"(lo));
      asm volatile("s_mov_b32 %0, cn_code_end-cn_k1" : "=s"(len));
      int off = (idx * 5 + rw) * 128;
      off = off + 64 < len ? off : 0;
      asm volatile("s_load_dword %0, %2, %3\n\ts_load_dword %1, %2, %4" : "=&s"(d0), "=&s"(d1) : "s"((const char*)pc + lo), "s"(off), "s"(off + 64) : "memory");
    }
    const char* src = isc ? (const char*)wc + rg * 8192 : (const char*)w2 + (64 * (s2 >> 1) + 16 * (r2 & 3) + 2 * (s2 & 1)) * 1024;
    src += coff;
    const int st2 = isc ? 2048 : 4096;
    v2u rt[8];
#pragma unroll
    for (int i = 0; i < 9; ++i) {
      if (i == R_AT) {
#pragma unroll
        for (int j = 0; j < 8; ++j) { asm volatile("global_load_dwordx2 %0, %1, off" : "=v"(rt[j]) : "v"(src + (j >> 1) * st2 + (j & 1) * 1024) : "memory"); }
      }
      BARRIER();
    }
    asm volatile("s_waitcnt vmcnt(0)" : "+v"(rt[0]), "+v"(rt[1]), "+v"(rt[2]), "+v"(rt[3]), "+v"(rt[4]), "+v"(rt[5]), "+v"(rt[6]), "+v"(rt[7]) :: "memory");
    const int fo = isc ? WS_WCT + ((((rg & 63) >> 2) * 9 + (rg >> 6)) * 2) * 1024 : WS_W2T + s2 * 4096;
    const int hs = isc ? 294912 : 2048;
    const int vo = fo + m * (isc ? 589824 : 65536) + ((rg & 3) * 16 + (lane & 15)) * 16;
    v4u o0, o1, o2, o3;
#pragma unroll
    for (int k = 0; k < 4; ++k) {
      o0[k] = __builtin_amdgcn_perm(rt[2 * k + 1].x, rt[2 * k].x, 0x05040100u); o1[k] = __builtin_amdgcn_perm(rt[2 * k + 1].x, rt[2 * k].x, 0x07060302u);
      o2[k] = __builtin_amdgcn_perm(rt[2 * k + 1].y, rt[2 * k].y, 0x05040100u); o3[k] = __builtin_amdgcn_perm(rt[2 * k + 1].y, rt[2 * k].y, 0x07060302u);
    }
    ST128(o0, rs, vo, SC1); ST128(o1, rs, vo + 1024, SC1); ST128(o2, rs, vo + hs, SC1); ST128(o3, rs, vo + hs + 1024, SC1);
    if (rw < 4) {
      const v4u po = {POISON, POISON, POISON, POISON};
      const int pob = WS_H2 + (idx >> 1) * 131072 + (bid & 7) * 16384 + (idx & 1) * 8192 + rw * 2048;
      ST128(po, rs, pob + lane * 16, SC1);
      ST128(po, rs, pob + 1024 + lane * 16, SC1);
    }
    asm volatile("s_waitcnt lgkmcnt(0)" : "+s"(d0), "+s"(d1) :: "memory");
  }
}

#define K2_L0 14
#define K2_RN 19
#define K2_RD 4
#define W2PRE 10
constexpr int K2_LDS = 131072;
constexpr int k2_min_lead() {
  constexpr int need[30] = {0, 0, 2, 1, 4, 5, 6, 7, 12, 14, 24, 26, 36, 38, 48, 49, 52, 53, 54, 55, 84, 86, 96, 97, 100, 101, 102, 103, 132, 134};
  int mn = 1000;
  for (int l = K2_L0; l < 120; ++l) { const int ld = (l / 30) * 144 + need[l % 30] - ((l - K2_L0) * K2_RN) / K2_RD; if (ld < mn) mn = ld; }
  return mn;
}
static_assert(k2_min_lead() >= 8, "a load would be issued behind its first use");

extern "C" __global__ __launch_bounds__(256, 1) void cn_k2(char* ws, const u16* __restrict__ xf, u16* __restrict__ out) {
  extern __shared__ __attribute__((aligned(16))) char smem_[];
  LAS char* smem = (LAS char*)smem_;
  const int bid = blockIdx.x, lane = threadIdx.x & 63, w = __builtin_amdgcn_readfirstlane(threadIdx.x >> 6);
  const int xcd = bid & 7, idx = bid >> 3, b = xcd >> 2, rt = idx >> 2, n = (xcd & 3) * 4 + (idx & 3);
  const __amdgpu_buffer_rsrc_t rs = __builtin_amdgcn_make_buffer_rsrc((void*)ws, 0, WS_END, RSRC_W3);
  const int vo = lane * 16;
  const int vo_top = rt == 0 ? 0x40000000 : vo, vo_bot = rt == 7 ? 0x40000000 : vo;
  const int wbase = WS_WCT + (n * 16 + 4 * w) * 18432;
  const int abase = WS_HN + 4 * w * 131072 + (b * 32 + 4 * rt - 1) * 2048;
  v4u W[4][18], A[4][12], S[12];
  f32x4 acc[4][2][2] = {};
  const int eoff = (n * 128 + (b * 32 + 4 * rt + w) * 2) * 1024;
  const int pt = xcd * 4 + (idx >> 3), ct = idx & 7;
  const int px = lane & 15, g = lane >> 4;
  const int ooff = (pt * 64 + 32 * (w >> 1) + 2 * px + (w & 1)) * 1024 + ct * 128 + g * 32;
  const int w2base = WS_W2T + ct * 65536 + 4 * w * 4096;
  v4u hin0, hin1, xv0, xv1, W2[4][4];
  auto ldx = [&](auto lc) AI {
    constexpr int l = decltype(lc)::v;
    if constexpr (l < 120) {
      constexpr int j = l / 30, i = l % 30;
      constexpr int wi[30] = {2, -1, -1, 3, 4, 5, 0, 1, -1, -1, -1, -1, -1, -1, 8, 9, 10, 11, 6, 7, -1, -1, 14, 15, 16, 17, 12, 13, -1, -1};
      constexpr int ai[30] = {-1, 0, 1, -1, -1, -1, -1, -1, 2, 3, 4, 5, 6, 7, -1, -1, -1, -1, -1, -1, 8, 9, -1, -1, -1, -1, -1, -1, 10, 11};
      if constexpr (wi[i] >= 0) { constexpr int f = wi[i]; W[j][f] = LD128(rs, vo + (f & 3) * 1024, wbase + j * 18432 + (f >> 2) * 4096, 0); }
      else { constexpr int f = ai[i]; A[j][f] = LD128(rs, (f < 2 ? vo_top : f >= 10 ? vo_bot : vo) + (f & 3) * 1024, abase + j * 131072 + (f >> 2) * 4096, 0); }
    }
    else if constexpr (l == 120) hin0 = LD128(rs, vo, WS_HIN + eoff, 0);
    else if constexpr (l == 121) hin1 = LD128(rs, vo + 1024, WS_HIN + eoff, 0);
    else if constexpr (l < 138) { constexpr int k = l - 122; W2[k >> 2][k & 3] = LD128(rs, vo + (k & 3) * 1024, w2base + (k >> 2) * 4096, 0); }
    else if constexpr (l == 138) xv0 = *(const GAS v4u*)((const char*)xf + ooff);
    else if constexpr (l == 139) xv1 = *(const GAS v4u*)((const char*)xf + ooff + 16);
  };
  auto ldsb = [&](auto lc) AI { ldx(lc); SB(); };
  static_for<0, K2_L0>(ldsb);
  constexpr int LREST = K2_L0 + (576 * K2_RD + K2_RN - 1) / K2_RN;
  LAS char* wscr = smem + w * 16384 + lane * 16;
  LAS char* rscr = smem + w * 4096 + lane * 16;
  static_for<0, 48>([&](auto uc) AI {
    constexpr int u = decltype(uc)::v, j = u / 12, dy = (u / 4) % 3, o = u % 4, R = o + dy;
    constexpr bool newrow = (dy == 0) || (o == 3);
    static_for<0, 12>([&](auto mc) AI {
      constexpr int m = decltype(mc)::v, slot = u * 12 + m;
      constexpr int dl = (slot * K2_RD + K2_RN - 1) / K2_RN;
      if constexpr ((dl * K2_RN) / K2_RD == slot) ldx(IC<K2_L0 + dl>{});
      if constexpr (newrow && m < 8) {
        constexpr int k = m & 3;
        if constexpr (m < 4) S[2 * R][k] = __builtin_amdgcn_update_dpp(0u, A[j][2 * R][k], 0x101, 0xf, 0xf, true);
        else S[2 * R + 1][k] = __builtin_amdgcn_update_dpp(0u, A[j][2 * R + 1][k], 0x111, 0xf, 0xf, true);
      }
      constexpr int c2 = m & 1, t = m >> 1;
      if constexpr (t == 0) acc[o][0][c2] = mfma16(W[j][(3 * dy + 1) * 2 + c2], A[j][2 * R], acc[o][0][c2]);
      if constexpr (t == 1) acc[o][1][c2] = mfma16(W[j][(3 * dy + 1) * 2 + c2], A[j][2 * R + 1], acc[o][1][c2]);
      if constexpr (t == 2) acc[o][0][c2] = mfma16(W[j][(3 * dy + 2) * 2 + c2], A[j][2 * R + 1], acc[o][0][c2]);
      if constexpr (t == 3) acc[o][1][c2] = mfma16(W[j][(3 * dy + 0) * 2 + c2], A[j][2 * R], acc[o][1][c2]);
      if constexpr (t == 4) acc[o][1][c2] = mfma16(W[j][(3 * dy + 2) * 2 + c2], S[2 * R], acc[o][1][c2]);
      if constexpr (t == 5) acc[o][0][c2] = mfma16(W[j][(3 * dy + 0) * 2 + c2], S[2 * R + 1], acc[o][0][c2]);
      SB();
      if constexpr (u > 44 && m >= 4 && m < 8) *(LAS f32x4*)(wscr + ((o - 1) * 4 + m - 4) * 1024) = acc[o - 1][(m - 4) >> 1][m & 1];
    });
  });
#pragma unroll
  for (int f = 0; f < 4; ++f) *(LAS f32x4*)(wscr + (12 + f) * 1024) = acc[3][f >> 1][f & 1];
#pragma unroll
  for (int f = 0; f < 12; ++f) asm volatile("" :: "a"(acc[f >> 2][(f >> 1) & 1][f & 1]));
  static_for<LREST, LREST + 3>(ldsb);
  wait_lgkm0();
  BARRIER();
  {
    f32x4 t[4][4];
#pragma unroll
    for (int f = 0; f < 4; ++f)
#pragma unroll
      for (int s = 0; s < 4; ++s) t[f][s] = *(LAS f32x4*)(rscr + s * 16384 + f * 1024);
    SB();
    static_for<LREST + 3, 140>(ldsb);
#pragma unroll
    for (int par = 0; par < 2; ++par) {
      const f32x4 t0 = (t[2 * par][0] + t[2 * par][1]) + (t[2 * par][2] + t[2 * par][3]);
      const f32x4 t1 = (t[2 * par + 1][0] + t[2 * par + 1][1]) + (t[2 * par + 1][2] + t[2 * par + 1][3]);
      const v4u hi = par ? hin1 : hin0;
      v4u o = {pk_bf16(t0[0] + bf_lo(hi.x), t1[0] + bf_hi(hi.x)), pk_bf16(t0[1] + bf_lo(hi.y), t1[1] + bf_hi(hi.y)),
               pk_bf16(t0[2] + bf_lo(hi.z), t1[2] + bf_hi(hi.z)), pk_bf16(t0[3] + bf_lo(hi.w), t1[3] + bf_hi(hi.w))};
#pragma unroll
      for (int i = 0; i < 4; ++i) o[i] = umin(o[i], POISON - 1u);
      ST128(o, rs, vo + par * 1024 + WS_H2 + eoff, SC1);
    }
  }
#pragma unroll
  for (int k = 0; k < 16; ++k) *(LAS v4u*)(smem + 65536 + w * 16384 + k * 1024 + lane * 16) = W2[k >> 2][k & 3];
  wait_lgkm0();
  BARRIER();
  {
    const int a2base = WS_H2 + pt * 4096 + w * 1024;
    f32x4 r[4];
#pragma unroll 1
    for (unsigned spin = 0;; ++spin) {
      CB();
      v4u A2[16], WF[16][4];
#pragma unroll
      for (int s = 0; s < 16; ++s) {
        A2[s] = LD128(rs, vo, a2base + s * 131072, SC1);
        if (s < W2PRE) {
#pragma unroll
          for (int t = 0; t < 4; ++t) WF[s][t] = *(LAS v4u*)(smem + 65536 + (s * 4 + t) * 1024 + lane * 16);
        }
        SB();
      }
      unsigned mx = 0;
#pragma unroll
      for (int s = 0; s < 16; ++s)
#pragma unroll
        for (int t = 0; t < 4; ++t) {
          const f32x4 zero = {0.f, 0.f, 0.f, 0.f};
          if (s + W2PRE < 16) WF[s + W2PRE][t] = *(LAS v4u*)(smem + 65536 + ((s + W2PRE) * 4 + t) * 1024 + lane * 16);
          r[t] = mfma16(WF[s][t], A2[s], s == 0 ? zero : r[t]);
          if (t == 0) mx = umax(umax(mx, A2[s].x), A2[s].y);
          if (t == 1) mx = umax(umax(mx, A2[s].z), A2[s].w);
          SB();
        }
      if (__builtin_expect(__all(mx != POISON), 1) || spin > SPIN_CAP) break;
    }
    v4u o0 = {pk_bf16(r[0][0] + bf_lo(xv0.x), r[1][0] + bf_hi(xv0.x)), pk_bf16(r[2][0] + bf_lo(xv0.y), r[3][0] + bf_hi(xv0.y)),
              pk_bf16(r[0][1] + bf_lo(xv0.z), r[1][1] + bf_hi(xv0.z)), pk_bf16(r[2][1] + bf_lo(xv0.w), r[3][1] + bf_hi(xv0.w))};
    v4u o1 = {pk_bf16(r[0][2] + bf_lo(xv1.x), r[1][2] + bf_hi(xv1.x)), pk_bf16(r[2][2] + bf_lo(xv1.y), r[3][2] + bf_hi(xv1.y)),
              pk_bf16(r[0][3] + bf_lo(xv1.z), r[1][3] + bf_hi(xv1.z)), pk_bf16(r[2][3] + bf_lo(xv1.w), r[3][3] + bf_hi(xv1.w))};
    *(GAS v4u*)((char*)out + ooff) = o0;
    *(GAS v4u*)((char*)out + ooff + 16) = o1;
  }
  asm volatile("cn_code_end:");
}

extern "C" void kernel_launch(void* const* d_in, const int* in_sizes, int n_in, void* d_out, int out_size, void* d_ws, size_t ws_size, hipStream_t stream) {
  const u16* xf = (const u16*)d_in[0]; const u16* x = (const u16*)d_in[1]; const u16* c = (const u16*)d_in[2];
  const u16* w1 = (const u16*)d_in[3]; const u16* w2 = (const u16*)d_in[4]; const u16* wc = (const u16*)d_in[5];
  char* ws = (char*)d_ws;
  (void)hipFuncSetAttribute((const void*)cn_k1, hipFuncAttributeMaxDynamicSharedMemorySize, K1_LDS);
  (void)hipFuncSetAttribute((const void*)cn_k2, hipFuncAttributeMaxDynamicSharedMemorySize, K2_LDS);
  cn_k1<<<256, 832, K1_LDS, stream>>>(x, c, w1, wc, w2, ws);
  cn_k2<<<256, 256, K2_LDS, stream>>>(ws, xf, (u16*)d_out);
}
```

```cpp
#include <hip/hip_runtime.h>
#include <hip/hip_bf16.h>
typedef unsigned short u16;
typedef unsigned v4u __attribute__((ext_vector_type(4)));
typedef unsigned v2u __attribute__((ext_vector_type(2)));
typedef short v4s __attribute__((ext_vector_type(4)));
typedef __bf16 v8bf __attribute__((ext_vector_type(8)));
typedef __bf16 v2bf __attribute__((ext_vector_type(2)));
typedef float f32x2 __attribute__((ext_vector_type(2)));
typedef float f32x4 __attribute__((ext_vector_type(4)));
#define GAS __attribute__((address_space(1)))
#define LAS __attribute__((address_space(3)))
#define DI __device__ __forceinline__
#define AI __attribute__((always_inline))
#define SB() __builtin_amdgcn_sched_barrier(0)
#define CB() asm volatile("" ::: "memory")

constexpr int WS_EPOCH = 0;
constexpr int WS_STATS = 16384;
constexpr int WS_HN = WS_STATS + 262144;
constexpr int WS_HIN = WS_HN + 2097152;
constexpr int WS_H2 = WS_HIN + 2097152;
constexpr int WS_WCT = WS_H2 + 2097152;
constexpr int WS_W2T = WS_WCT + 4718592;
constexpr int WS_END = WS_W2T + 524288;
constexpr int RSRC_W3 = 0x00020000;
constexpr int SC1 = 16;
constexpr unsigned SPIN_CAP = 1u << 17;
constexpr unsigned POISON = 0xFFFFFFFFu;

DI float asf(unsigned u) { return __builtin_bit_cast(float, u); }
DI unsigned asu(float f) { return __builtin_bit_cast(unsigned, f); }
DI unsigned pk_bf16(float a, float b) { f32x2 v = {a, b}; return __builtin_bit_cast(unsigned, __builtin_convertvector(v, v2bf)); }
DI float bf_lo(unsigned u) { return asf(u << 16); }
DI float bf_hi(unsigned u) { return asf(u & 0xffff0000u); }
DI unsigned umin(unsigned a, unsigned b) { return a < b ? a : b; }
DI unsigned umax(unsigned a, unsigned b) { return a > b ? a : b; }
DI f32x4 mfma16(v4u a, v4u b, f32x4 c) { return __builtin_amdgcn_mfma_f32_16x16x32_bf16(__builtin_bit_cast(v8bf, a), __builtin_bit_cast(v8bf, b), c, 0, 0, 0); }
DI float xsum16(float v) { auto r = __builtin_amdgcn_permlane16_swap(asu(v), asu(v), false, false); return asf(r[0]) + asf(r[1]); }
DI float xsum32(float v) { auto r = __builtin_amdgcn_permlane32_swap(asu(v), asu(v), false, false); return asf(r[0]) + asf(r[1]); }
DI v4u widen16(v2u a, v2u b) {
  auto rx = __builtin_amdgcn_permlane16_swap(a.x, b.x, false, false);
  auto ry = __builtin_amdgcn_permlane16_swap(a.y, b.y, false, false);
  v4u o = {rx[0], ry[0], rx[1], ry[1]};
  return o;
}
DI v4u transpose4(unsigned a0, unsigned a1, unsigned a2, unsigned a3) {
  auto p = __builtin_amdgcn_permlane16_swap(a0, a1, false, false);
  auto q = __builtin_amdgcn_permlane16_swap(a2, a3, false, false);
  auto r = __builtin_amdgcn_permlane32_swap(p[0], q[0], false, false);
  auto s = __builtin_amdgcn_permlane32_swap(p[1], q[1], false, false);
  v4u o = {r[0], s[0], r[1], s[1]};
  return o;
}
DI v4u trfrag(LAS char* p0, LAS char* p1) {
  v4s a0 = __builtin_amdgcn_ds_read_tr16_b64_v4i16((LAS v4s*)p0);
  v4s a1 = __builtin_amdgcn_ds_read_tr16_b64_v4i16((LAS v4s*)p1);
  return __builtin_bit_cast(v4u, __builtin_shufflevector(a0, a1, 0, 1, 2, 3, 4, 5, 6, 7));
}
template <int V> struct IC { static constexpr int v = V; };
template <int B, int E, class F> DI void static_for(F&& f) { if constexpr (B < E) { f(IC<B>{}); static_for<B + 1, E>(f); } }
#define BARRIER() do { CB(); __builtin_amdgcn_s_barrier(); CB(); } while (0)
template <int N> DI void wait_vm() { asm volatile("s_waitcnt vmcnt(%0)" ::"i"(N) : "memory"); }
DI void wait_lgkm0() { asm volatile("s_waitcnt lgkmcnt(0)" ::: "memory"); }
DI v4u mk_rsrc(const void* p, unsigned bytes) { unsigned long long a = (unsigned long long)p; v4u r = {(unsigned)a, (unsigned)(a >> 32) & 0xffffu, bytes, (unsigned)RSRC_W3}; return r; }
DI void dma16(v4u rsrc, int voff, int soff, int lds) {
  asm volatile("s_mov_b32 m0, %0\n\ts_nop 0\n\tbuffer_load_dwordx4 %1, %2, %3 offen lds" ::"s"(lds), "v"(voff), "s"(rsrc), "s"(soff) : "memory");
}
#define ST128(val, rs, v, aux) __builtin_amdgcn_raw_buffer_store_b128(val, rs, v, 0, aux)
#define LD128(rs, v, s, aux) __builtin_amdgcn_raw_buffer_load_b128(rs, v, s, aux)
#define ST32(val, rs, v, aux) __builtin_amdgcn_raw_buffer_store_b32(val, rs, v, 0, aux)

constexpr int L_C = 0;
constexpr int L_W = 65536;
constexpr int L_X = 131072;
constexpr int K1_LDS = 139264;
#define R_AT 4
#define XPRE 16
extern "C" __global__ __launch_bounds__(832, 4) void cn_k1(const u16* __restrict__ x, const u16* __restrict__ c, const u16* __restrict__ w1,
                                                           const u16* __restrict__ wc, const u16* __restrict__ w2, char* ws) {
  extern __shared__ __attribute__((aligned(16))) char smem_[];
  LAS char* smem = (LAS char*)smem_;
  const int bid = blockIdx.x, lane = threadIdx.x & 63, wv = __builtin_amdgcn_readfirstlane(threadIdx.x >> 6);
  const int idx = bid >> 3, pt = (bid & 7) * 4 + (idx >> 3), ct = idx & 7;
  const __amdgpu_buffer_rsrc_t rs = __builtin_amdgcn_make_buffer_rsrc((void*)ws, 0, WS_END, RSRC_W3);
  if (__builtin_expect(wv < 8, 1)) {
    const int w = wv & 3;
    const int r8 = lane >> 3, s8 = lane & 7;
    const int voc = (pt * 64 + (w >> 1) + 2 * (8 * (w & 1) + r8)) * 1024 + ((s8 ^ (4 * (w & 1) + (r8 >> 1))) << 4);
    const int vow = (8 * w + r8) * 1024 + ct * 128 + ((s8 ^ (r8 & 2) ^ ((w & 1) << 2)) << 4);
    const int rot = 4 * ((ct ^ (idx >> 3)) & 1);
    const v4u rc = mk_rsrc(c, 2097152), rw1 = mk_rsrc(w1, 524288);
    {
      const int hi = (wv >> 2) * 32768;
#pragma unroll
      for (int i = 0; i < 4; ++i) {
        const int kc = i ^ rot;
        dma16(rc, voc, hi + kc * 128, L_C + i * 8192 + wv * 1024);
        dma16(rw1, vow, hi + kc * 65536, L_W + i * 8192 + wv * 1024);
      }
    }
    if (__builtin_expect(wv >= 4, 1)) {
      auto issue = [&](int i) {
        const int kc = i ^ rot;
        dma16(rc, voc, kc * 128, L_C + i * 8192 + w * 1024);
        dma16(rw1, vow, kc * 65536, L_W + i * 8192 + w * 1024);
        dma16(rc, voc, 32768 + kc * 128, L_C + i * 8192 + 4096 + w * 1024);
        dma16(rw1, vow, 32768 + kc * 65536, L_W + i * 8192 + 4096 + w * 1024);
      };
      const v4u rx = mk_rsrc(x, 2097152);
      dma16(rx, voc, ct * 128, L_X + w * 1024);
      dma16(rx, voc, 32768 + ct * 128, L_X + 4096 + w * 1024);
      wait_vm<8>(); BARRIER(); issue(4);
      wait_vm<10>(); BARRIER(); issue(5);
      wait_vm<12>(); BARRIER(); issue(6);
      wait_vm<14>(); BARRIER(); issue(7);
      wait_vm<12>(); BARRIER();
      wait_vm<8>(); BARRIER();
      wait_vm<4>(); BARRIER();
      wait_vm<0>(); BARRIER();
      return;
    }
    const int px = lane & 15, g = lane >> 4, q = (lane & 15) >> 2, p = lane & 3;
    const int row = 16 * w + px;
    const int soff = WS_STATS + pt * 8192 + row * 16;
    unsigned epoch_;
    asm volatile("s_load_dword %0, %1, %2" : "=&s"(epoch_) : "s"(ws), "s"(WS_EPOCH + (pt * 4 + w) * 128) : "memory");
    LAS char* cb0 = smem + L_C + row * 128 + ((g ^ ((px >> 1) & 7)) << 4);
    LAS char* cb1 = smem + L_C + row * 128 + (((4 + g) ^ ((px >> 1) & 7)) << 4);
    LAS char* wb[4];
#pragma unroll
    for (int cc = 0; cc < 4; ++cc) wb[cc] = smem + L_W + (8 * g + q) * 128 + (((2 * cc + (p >> 1)) ^ ((q & 2) | ((g & 1) << 2))) << 4) + 8 * (p & 1);
    f32x4 acc[4] = {};
    v4u bq[2][2], aq[2][2][4];
    v2u xv[4];
#pragma unroll
    for (int kc = 0; kc <= 8; ++kc) {
      if (kc == 0) { asm volatile("s_waitcnt vmcnt(6) lgkmcnt(0)" : "+s"(epoch_) :: "memory"); }
      if (kc == 1) wait_vm<4>();
      if (kc == 2) wait_vm<2>();
      if (kc == 3) wait_vm<0>();
      if (kc < 8) BARRIER();
#pragma unroll
      for (int s = 0; s < 2; ++s) {
        if (kc < 8) bq[kc & 1][s] = *(LAS v4u*)((s ? cb1 : cb0) + kc * 8192);
#pragma unroll
        for (int cc = 0; cc < 4; ++cc) {
          if (kc < 8) aq[kc & 1][s][cc] = trfrag(wb[cc] + kc * 8192 + s * 4096, wb[cc] + kc * 8192 + s * 4096 + 512);
          if (kc == 5 && s == 1) xv[cc] = *(LAS v2u*)(smem + L_X + row * 128 + (((2 * cc + (g >> 1)) ^ ((px >> 1) & 7)) << 4) + 8 * (g & 1));
          if (kc > 0) acc[cc] = mfma16(aq[(kc - 1) & 1][s][cc], bq[(kc - 1) & 1][s], acc[cc]);
          SB();
        }
      }
      if (kc == 6) {
#pragma unroll
        for (int cc = 0; cc < 4; ++cc) { acc[cc][0] += bf_lo(xv[cc].x); acc[cc][1] += bf_hi(xv[cc].x); acc[cc][2] += bf_lo(xv[cc].y); acc[cc][3] += bf_hi(xv[cc].y); }
        SB();
      }
    }
    const unsigned tag = epoch_ + 1u;
    float v[16]; v2u hin[4];
    float sa = 0.f, sb = 0.f, qa = 0.f, qb = 0.f;
#pragma unroll
    for (int cc = 0; cc < 4; ++cc) {
      hin[cc].x = pk_bf16(acc[cc][0], acc[cc][1]);
      hin[cc].y = pk_bf16(acc[cc][2], acc[cc][3]);
      v[4 * cc] = bf_lo(hin[cc].x); v[4 * cc + 1] = bf_hi(hin[cc].x); v[4 * cc + 2] = bf_lo(hin[cc].y); v[4 * cc + 3] = bf_hi(hin[cc].y);
      sa += v[4 * cc] + v[4 * cc + 2]; sb += v[4 * cc + 1] + v[4 * cc + 3];
      qa += v[4 * cc] * v[4 * cc]; qb += v[4 * cc + 1] * v[4 * cc + 1]; qa += v[4 * cc + 2] * v[4 * cc + 2]; qb += v[4 * cc + 3] * v[4 * cc + 3];
    }
    const float s64 = xsum32(xsum16(sa + sb)), q64 = xsum32(xsum16(qa + qb));
    if (g == 0) { v4u gr = {asu(s64), tag, asu(q64), tag}; ST128(gr, rs, soff + ct * 1024, SC1); }
    SB();
    const int fbase = (2 * ct * 128 + 4 * pt + w) * 1024;
    const v4u hin_a = transpose4(hin[0].x, hin[1].x, hin[2].x, hin[3].x), hin_b = transpose4(hin[0].y, hin[1].y, hin[2].y, hin[3].y);
    v4u g0, g1;
    __builtin_amdgcn_s_sleep(XPRE);
    for (unsigned spin = 0;; ++spin) {
      CB();
      g0 = LD128(rs, soff + g * 2048, 0, SC1); g1 = LD128(rs, soff + g * 2048 + 1024, 0, SC1);
      if (__all((g0.y == tag) & (g0.w == tag) & (g1.y == tag) & (g1.w == tag)) || spin > SPIN_CAP) break;
    }
    ST128(hin_a, rs, lane * 16 + WS_HIN + fbase, SC1);
    ST128(hin_b, rs, lane * 16 + WS_HIN + fbase + 131072, SC1);
    if (ct == 0 && lane == 0) ST32(tag, rs, WS_EPOCH + (pt * 4 + w) * 128, SC1);
    const float mean = xsum32(xsum16(asf(g0.x) + asf(g1.x))) * (1.f / 512.f);
    const float ex2 = xsum32(xsum16(asf(g0.z) + asf(g1.z))) * (1.f / 512.f);
    const float rstd = __builtin_amdgcn_rsqf(fmaxf(ex2 - mean * mean, 0.f) + 1e-5f);
    const float nb = -mean * rstd;
    const float rs2 = rstd * -1.44269504f, nb2 = nb * -1.44269504f;
    const int poff = (((g >> 1) + 2 * (g & 1)) * 16 + px) * 16;
    v2u hv[4];
#pragma unroll
    for (int cc = 0; cc < 4; ++cc) {
      float o[4];
#pragma unroll
      for (int i = 0; i < 4; ++i) { const float y = v[4 * cc + i] * rstd + nb; o[i] = y * __builtin_amdgcn_rcpf(1.f + __builtin_amdgcn_exp2f(v[4 * cc + i] * rs2 + nb2)); }
      hv[cc].x = pk_bf16(o[0], o[1]); hv[cc].y = pk_bf16(o[2], o[3]);
      if (cc & 1) ST128(widen16(hv[cc - 1], hv[cc]), rs, poff + WS_HN + fbase + (cc >> 1) * 131072, SC1);
    }
    return;
  }
  {
    const int rw = wv - 8;
    const int u = bid * 5 + rw, rg = u >> 1, m = 4 * (u & 1) + (lane >> 4);
    const int coff = (u & 1) * 512 + lane * 8;
    const bool isc = rg < 576;
    const int r2 = rg - 576, s2 = r2 >> 2;
    unsigned d0, d1;
    {
      unsigned long long pc; asm volatile("s_getpc_b64 %0\ncn_code_here:" : "=s"(pc));
      int lo, len;
      asm volatile("s_mov_b32 %0, cn_k1-cn_code_here" : "=s"(lo));
      asm volatile("s_mov_b32 %0, cn_code_end-cn_k1" : "=s"(len));
      int off = (idx * 5 + rw) * 128;
      off = off + 64 < len ? off : 0;
      asm volatile("s_load_dword %0, %2, %3\n\ts_load_dword %1, %2, %4" : "=&s"(d0), "=&s"(d1) : "s"((const char*)pc + lo), "s"(off), "s"(off + 64) : "memory");
    }
    const char* src = isc ? (const char*)wc + rg * 8192 : (const char*)w2 + (64 * (s2 >> 1) + 16 * (r2 & 3) + 2 * (s2 & 1)) * 1024;
    src += coff;
    const int st2 = isc ? 2048 : 4096;
    v2u rt[8];
#pragma unroll
    for (int i = 0; i < 8; ++i) {
      if (i == R_AT) {
#pragma unroll
        for (int j = 0; j < 8; ++j) { asm volatile("global_load_dwordx2 %0, %1, off" : "=v"(rt[j]) : "v"(src + (j >> 1) * st2 + (j & 1) * 1024) : "memory"); }
        if (rw < 4) {
          const v4u po = {POISON, POISON, POISON, POISON};
          const int pob = WS_H2 + (idx >> 1) * 131072 + (bid & 7) * 16384 + (idx & 1) * 8192 + rw * 2048;
          ST128(po, rs, pob + lane * 16, SC1);
          ST128(po, rs, pob + 1024 + lane * 16, SC1);
        }
        SB();
      }
      BARRIER();
    }
    if (rw == 4) wait_vm<0>();
    asm volatile("s_waitcnt vmcnt(2)" : "+v"(rt[0]), "+v"(rt[1]), "+v"(rt[2]), "+v"(rt[3]), "+v"(rt[4]), "+v"(rt[5]), "+v"(rt[6]), "+v"(rt[7]) :: "memory");
    const int fo = isc ? WS_WCT + ((((rg & 63) >> 2) * 9 + (rg >> 6)) * 2) * 1024 : WS_W2T + s2 * 4096;
    const int hs = isc ? 294912 : 2048;
    const int vo = fo + m * (isc ? 589824 : 65536) + ((rg & 3) * 16 + (lane & 15)) * 16;
    v4u o0, o1, o2, o3;
#pragma unroll
    for (int k = 0; k < 4; ++k) {
      o0[k] = __builtin_amdgcn_perm(rt[2 * k + 1].x, rt[2 * k].x, 0x05040100u); o1[k] = __builtin_amdgcn_perm(rt[2 * k + 1].x, rt[2 * k].x, 0x07060302u);
      o2[k] = __builtin_amdgcn_perm(rt[2 * k + 1].y, rt[2 * k].y, 0x05040100u); o3[k] = __builtin_amdgcn_perm(rt[2 * k + 1].y, rt[2 * k].y, 0x07060302u);
    }
    ST128(o0, rs, vo, SC1); ST128(o1, rs, vo + 1024, SC1); ST128(o2, rs, vo + hs, SC1); ST128(o3, rs, vo + hs + 1024, SC1);
    asm volatile("s_waitcnt lgkmcnt(0)" : "+s"(d0), "+s"(d1) :: "memory");
  }
}

#define K2_L0 14
#define K2_RN 19
#define K2_RD 4
#define W2PRE 10
constexpr int K2_LDS = 131072;
constexpr int k2_min_lead() {
  constexpr int need[30] = {0, 0, 2, 1, 4, 5, 6, 7, 12, 14, 24, 26, 36, 38, 48, 49, 52, 53, 54, 55, 84, 86, 96, 97, 100, 101, 102, 103, 132, 134};
  int mn = 1000;
  for (int l = K2_L0; l < 120; ++l) { const int ld = (l / 30) * 144 + need[l % 30] - ((l - K2_L0) * K2_RN) / K2_RD; if (ld < mn) mn = ld; }
  return mn;
}
static_assert(k2_min_lead() >= 8, "a load would be issued behind its first use");

extern "C" __global__ __launch_bounds__(256, 1) void cn_k2(char* ws, const u16* __restrict__ xf, u16* __restrict__ out) {
  extern __shared__ __attribute__((aligned(16))) char smem_[];
  LAS char* smem = (LAS char*)smem_;
  const int bid = blockIdx.x, lane = threadIdx.x & 63, w = __builtin_amdgcn_readfirstlane(threadIdx.x >> 6);
  const int xcd = bid & 7, idx = bid >> 3, b = xcd >> 2, rt = idx >> 2, n = (xcd & 3) * 4 + (idx & 3);
  const __amdgpu_buffer_rsrc_t rs = __builtin_amdgcn_make_buffer_rsrc((void*)ws, 0, WS_END, RSRC_W3);
  const int vo = lane * 16;
  const int vo_top = rt == 0 ? 0x40000000 : vo, vo_bot = rt == 7 ? 0x40000000 : vo;
  const int wbase = WS_WCT + (n * 16 + 4 * w) * 18432;
  const int abase = WS_HN + 4 * w * 131072 + (b * 32 + 4 * rt - 1) * 2048;
  v4u W[4][18], A[4][12], S[12];
  f32x4 acc[4][2][2] = {};
  const int eoff = (n * 128 + (b * 32 + 4 * rt + w) * 2) * 1024;
  const int pt = xcd * 4 + (idx >> 3), ct = idx & 7;
  const int px = lane & 15, g = lane >> 4;
  const int ooff = (pt * 64 + 32 * (w >> 1) + 2 * px + (w & 1)) * 1024 + ct * 128 + g * 32;
  const int w2base = WS_W2T + ct * 65536 + 4 * w * 4096;
  v4u hin0, hin1, xv0, xv1, W2[4][4];
  auto ldx = [&](auto lc) AI {
    constexpr int l = decltype(lc)::v;
    if constexpr (l < 120) {
      constexpr int j = l / 30, i = l % 30;
      constexpr int wi[30] = {2, -1, -1, 3, 4, 5, 0, 1, -1, -1, -1, -1, -1, -1, 8, 9, 10, 11, 6, 7, -1, -1, 14, 15, 16, 17, 12, 13, -1, -1};
      constexpr int ai[30] = {-1, 0, 1, -1, -1, -1, -1, -1, 2, 3, 4, 5, 6, 7, -1, -1, -1, -1, -1, -1, 8, 9, -1, -1, -1, -1, -1, -1, 10, 11};
      if constexpr (wi[i] >= 0) { constexpr int f = wi[i]; W[j][f] = LD128(rs, vo + (f & 3) * 1024, wbase + j * 18432 + (f >> 2) * 4096, 0); }
      else { constexpr int f = ai[i]; A[j][f] = LD128(rs, (f < 2 ? vo_top : f >= 10 ? vo_bot : vo) + (f & 3) * 1024, abase + j * 131072 + (f >> 2) * 4096, 0); }
    }
    else if constexpr (l == 120) hin0 = LD128(rs, vo, WS_HIN + eoff, 0);
    else if constexpr (l == 121) hin1 = LD128(rs, vo + 1024, WS_HIN + eoff, 0);
    else if constexpr (l < 138) { constexpr int k = l - 122; W2[k >> 2][k & 3] = LD128(rs, vo + (k & 3) * 1024, w2base + (k >> 2) * 4096, 0); }
    else if constexpr (l == 138) xv0 = *(const GAS v4u*)((const char*)xf + ooff);
    else if constexpr (l == 139) xv1 = *(const GAS v4u*)((const char*)xf + ooff + 16);
  };
  auto ldsb = [&](auto lc) AI { ldx(lc); SB(); };
  static_for<0, K2_L0>(ldsb);
  constexpr int LREST = K2_L0 + (576 * K2_RD + K2_RN - 1) / K2_RN;
  LAS char* wscr = smem + w * 16384 + lane * 16;
  LAS char* rscr = smem + w * 4096 + lane * 16;
  static_for<0, 48>([&](auto uc) AI {
    constexpr int u = decltype(uc)::v, j = u / 12, dy = (u / 4) % 3, o = u % 4, R = o + dy;
    constexpr bool newrow = (dy == 0) || (o == 3);
    static_for<0, 12>([&](auto mc) AI {
      constexpr int m = decltype(mc)::v, slot = u * 12 + m;
      constexpr int dl = (slot * K2_RD + K2_RN - 1) / K2_RN;
      if constexpr ((dl * K2_RN) / K2_RD == slot) ldx(IC<K2_L0 + dl>{});
      if constexpr (newrow && m < 8) {
        constexpr int k = m & 3;
        if constexpr (m < 4) S[2 * R][k] = __builtin_amdgcn_update_dpp(0u, A[j][2 * R][k], 0x101, 0xf, 0xf, true);
        else S[2 * R + 1][k] = __builtin_amdgcn_update_dpp(0u, A[j][2 * R + 1][k], 0x111, 0xf, 0xf, true);
      }
      constexpr int c2 = m & 1, t = m >> 1;
      if constexpr (t == 0) acc[o][0][c2] = mfma16(W[j][(3 * dy + 1) * 2 + c2], A[j][2 * R], acc[o][0][c2]);
      if constexpr (t == 1) acc[o][1][c2] = mfma16(W[j][(3 * dy + 1) * 2 + c2], A[j][2 * R + 1], acc[o][1][c2]);
      if constexpr (t == 2) acc[o][0][c2] = mfma16(W[j][(3 * dy + 2) * 2 + c2], A[j][2 * R + 1], acc[o][0][c2]);
      if constexpr (t == 3) acc[o][1][c2] = mfma16(W[j][(3 * dy + 0) * 2 + c2], A[j][2 * R], acc[o][1][c2]);
      if constexpr (t == 4) acc[o][1][c2] = mfma16(W[j][(3 * dy + 2) * 2 + c2], S[2 * R], acc[o][1][c2]);
      if constexpr (t == 5) acc[o][0][c2] = mfma16(W[j][(3 * dy + 0) * 2 + c2], S[2 * R + 1], acc[o][0][c2]);
      SB();
      if constexpr (u > 44 && m >= 4 && m < 8) *(LAS f32x4*)(wscr + ((o - 1) * 4 + m - 4) * 1024) = acc[o - 1][(m - 4) >> 1][m & 1];
    });
  });
#pragma unroll
  for (int f = 0; f < 4; ++f) *(LAS f32x4*)(wscr + (12 + f) * 1024) = acc[3][f >> 1][f & 1];
#pragma unroll
  for (int f = 0; f < 12; ++f) asm volatile("" :: "a"(acc[f >> 2][(f >> 1) & 1][f & 1]));
  static_for<LREST, LREST + 3>(ldsb);
  wait_lgkm0();
  BARRIER();
  {
    f32x4 t[4][4];
#pragma unroll
    for (int f = 0; f < 4; ++f)
#pragma unroll
      for (int s = 0; s < 4; ++s) t[f][s] = *(LAS f32x4*)(rscr + s * 16384 + f * 1024);
    SB();
    static_for<LREST + 3, 140>(ldsb);
#pragma unroll
    for (int par = 0; par < 2; ++par) {
      const f32x4 t0 = (t[2 * par][0] + t[2 * par][1]) + (t[2 * par][2] + t[2 * par][3]);
      const f32x4 t1 = (t[2 * par + 1][0] + t[2 * par + 1][1]) + (t[2 * par + 1][2] + t[2 * par + 1][3]);
      const v4u hi = par ? hin1 : hin0;
      v4u o = {pk_bf16(t0[0] + bf_lo(hi.x), t1[0] + bf_hi(hi.x)), pk_bf16(t0[1] + bf_lo(hi.y), t1[1] + bf_hi(hi.y)),
               pk_bf16(t0[2] + bf_lo(hi.z), t1[2] + bf_hi(hi.z)), pk_bf16(t0[3] + bf_lo(hi.w), t1[3] + bf_hi(hi.w))};
#pragma unroll
      for (int i = 0; i < 4; ++i) o[i] = umin(o[i], POISON - 1u);
      ST128(o, rs, vo + par * 1024 + WS_H2 + eoff, SC1);
    }
  }
#pragma unroll
  for (int k = 0; k < 16; ++k) *(LAS v4u*)(smem + 65536 + w * 16384 + k * 1024 + lane * 16) = W2[k >> 2][k & 3];
  wait_lgkm0();
  BARRIER();
  {
    const int a2base = WS_H2 + pt * 4096 + w * 1024;
    f32x4 r[4];
#pragma unroll 1
    for (unsigned spin = 0;; ++spin) {
      CB();
      v4u A2[16], WF[16][4];
#pragma unroll
      for (int s = 0; s < 16; ++s) {
        A2[s] = LD128(rs, vo, a2base + s * 131072, SC1);
        if (s < W2PRE) {
#pragma unroll
          for (int t = 0; t < 4; ++t) WF[s][t] = *(LAS v4u*)(smem + 65536 + (s * 4 + t) * 1024 + lane * 16);
        }
        SB();
      }
      unsigned mx = 0;
#pragma unroll
      for (int s = 0; s < 16; ++s)
#pragma unroll
        for (int t = 0; t < 4; ++t) {
          const f32x4 zero = {0.f, 0.f, 0.f, 0.f};
          if (s + W2PRE < 16) WF[s + W2PRE][t] = *(LAS v4u*)(smem + 65536 + ((s + W2PRE) * 4 + t) * 1024 + lane * 16);
          r[t] = mfma16(WF[s][t], A2[s], s == 0 ? zero : r[t]);
          if (t == 0) mx = umax(umax(mx, A2[s].x), A2[s].y);
          if (t == 1) mx = umax(umax(mx, A2[s].z), A2[s].w);
          SB();
        }
      if (__builtin_expect(__all(mx != POISON), 1) || spin > SPIN_CAP) break;
    }
    v4u o0 = {pk_bf16(r[0][0] + bf_lo(xv0.x), r[1][0] + bf_hi(xv0.x)), pk_bf16(r[2][0] + bf_lo(xv0.y), r[3][0] + bf_hi(xv0.y)),
              pk_bf16(r[0][1] + bf_lo(xv0.z), r[1][1] + bf_hi(xv0.z)), pk_bf16(r[2][1] + bf_lo(xv0.w), r[3][1] + bf_hi(xv0.w))};
    v4u o1 = {pk_bf16(r[0][2] + bf_lo(xv1.x), r[1][2] + bf_hi(xv1.x)), pk_bf16(r[2][2] + bf_lo(xv1.y), r[3][2] + bf_hi(xv1.y)),
              pk_bf16(r[0][3] + bf_lo(xv1.z), r[1][3] + bf_hi(xv1.z)), pk_bf16(r[2][3] + bf_lo(xv1.w), r[3][3] + bf_hi(xv1.w))};
    *(GAS v4u*)((char*)out + ooff) = o0;
    *(GAS v4u*)((char*)out + ooff + 16) = o1;
  }
  asm volatile("cn_code_end:");
}

extern "C" void kernel_launch(void* const* d_in, const int* in_sizes, int n_in, void* d_out, int out_size, void* d_ws, size_t ws_size, hipStream_t stream) {
  const u16* xf = (const u16*)d_in[0]; const u16* x = (const u16*)d_in[1]; const u16* c = (const u16*)d_in[2];
  const u16* w1 = (const u16*)d_in[3]; const u16* w2 = (const u16*)d_in[4]; const u16* wc = (const u16*)d_in[5];
  char* ws = (char*)d_ws;
  (void)hipFuncSetAttribute((const void*)cn_k1, hipFuncAttributeMaxDynamicSharedMemorySize, K1_LDS);
  (void)hipFuncSetAttribute((const void*)cn_k2, hipFuncAttributeMaxDynamicSharedMemorySize, K2_LDS);
  cn_k1<<<256, 832, K1_LDS, stream>>>(x, c, w1, wc, w2, ws);
  cn_k2<<<256, 256, K2_LDS, stream>>>(ws, xf, (u16*)d_out);
}
```
